```python
import torch

def _softmax(x, axis=-1):
    return torch.softmax(x, dim=axis)
def _gelu(x):
    return torch.nn.functional.gelu(x, approximate="tanh")
def _silu(x):
    return torch.nn.functional.silu(x)
def _sigmoid(x):
    return torch.sigmoid(x)
def _layernorm(x, axis=-1, eps=1e-5):
    m = x.mean(dim=axis, keepdim=True)
    v = x.var(dim=axis, keepdim=True, unbiased=False)
    return (x - m) / torch.sqrt(v + eps)
def _scatter_add(tgt, idx, src, axis=0):
    return tgt.clone().index_add_(axis, idx, src)
def _segment_sum(src, seg, n):
    out = torch.zeros((n,) + src.shape[1:], dtype=src.dtype)
    return out.index_add_(0, seg, src)
def _pad(x, pw):
    flat = tuple(v for pair in reversed(pw) for v in pair)
    return torch.nn.functional.pad(x, flat)

kernel_name = 'concat_heads__gqa_block_cached__gqa_block_cached__near_ridge'

def setup_inputs():

    _w = (lambda a: (lambda t: t.to(torch.bfloat16) if t.is_floating_point() else t)(torch.as_tensor(a)))
    def _A_setup():
        B, Sq, D = 8, 144, 640
        Hq, Hkv, Sk, Dh = 32, 32, 1024, 128
        assert Hq % Hkv == 0, (Hq, Hkv)
        return {
            "x": _w((lambda *s: torch.randn(*s, dtype=torch.bfloat16) * 0.8)(B, Sq, D)),
            "k_cache": _w((lambda *s: torch.randn(*s, dtype=torch.bfloat16) * 0.8)(B, Hkv, Sk, Dh)),
            "v_cache": _w((lambda *s: torch.randn(*s, dtype=torch.bfloat16) * 0.05)(B, Hkv, Sk, Dh)),
            "wq": _w((lambda *s: torch.randn(*s, dtype=torch.bfloat16) * 0.8)(D, Hq * Dh) * (D ** -0.5)),
            "wo": _w((lambda *s: torch.randn(*s, dtype=torch.bfloat16) * 0.05)(Hq * Dh, D) * ((Hq * Dh) ** -0.5)),
        }
    _A_in = _A_setup()

    def _B_setup():
        B, Sq, D = 8, 144, 640
        Hq, Hkv, Sk, Dh = 32, 32, 1024, 128
        assert Hq % Hkv == 0, (Hq, Hkv)
        return {
            "x": _w((lambda *s: torch.randn(*s, dtype=torch.bfloat16) * 0.8)(B, Sq, D)),
            "k_cache": _w((lambda *s: torch.randn(*s, dtype=torch.bfloat16) * 0.8)(B, Hkv, Sk, Dh)),
            "v_cache": _w((lambda *s: torch.randn(*s, dtype=torch.bfloat16) * 0.05)(B, Hkv, Sk, Dh)),
            "wq": _w((lambda *s: torch.randn(*s, dtype=torch.bfloat16) * 0.8)(D, Hq * Dh) * (D ** -0.5)),
            "wo": _w((lambda *s: torch.randn(*s, dtype=torch.bfloat16) * 0.05)(Hq * Dh, D) * ((Hq * Dh) ** -0.5)),
        }
    _B_in = _B_setup()

    return {
        "A_x": _A_in["x"],
        "A_k_cache": _A_in["k_cache"],
        "A_v_cache": _A_in["v_cache"],
        "A_wq": _A_in["wq"],
        "A_wo": _A_in["wo"],
        "B_x": _B_in["x"],
        "B_k_cache": _B_in["k_cache"],
        "B_v_cache": _B_in["v_cache"],
        "B_wq": _B_in["wq"],
        "B_wo": _B_in["wo"],
    }

def reference(A_x, A_k_cache, A_v_cache, A_wq, A_wo, B_x, B_k_cache, B_v_cache, B_wq, B_wo):
    def _A_ref(x, k_cache, v_cache, wq, wo):
        B, Sq, D = x.shape
        Hq, Dh = 32, 128
        Hkv = k_cache.shape[1]
        rep = Hq // Hkv
        q = (lambda x, *d: x.permute(*d))((x @ wq).reshape(B, Sq, Hq, Dh), 0, 2, 1, 3)
        k = (lambda x, n, axis: torch.repeat_interleave(x, n, dim=axis))(k_cache, rep, axis=1)
        v = (lambda x, n, axis: torch.repeat_interleave(x, n, dim=axis))(v_cache, rep, axis=1)
        scores = torch.einsum("bhqd,bhkd->bhqk", q, k) * (Dh ** -0.5)
        p = _softmax(scores, axis=-1)
        o = torch.einsum("bhqk,bhkd->bhqd", p, v)
        return (lambda x, *d: x.permute(*d))(o, 0, 2, 1, 3).reshape(B, Sq, Hq * Dh) @ wo

    def _B_ref(x, k_cache, v_cache, wq, wo):
        B, Sq, D = x.shape
        Hq, Dh = 32, 128
        Hkv = k_cache.shape[1]
        rep = Hq // Hkv
        q = (lambda x, *d: x.permute(*d))((x @ wq).reshape(B, Sq, Hq, Dh), 0, 2, 1, 3)
        k = (lambda x, n, axis: torch.repeat_interleave(x, n, dim=axis))(k_cache, rep, axis=1)
        v = (lambda x, n, axis: torch.repeat_interleave(x, n, dim=axis))(v_cache, rep, axis=1)
        scores = torch.einsum("bhqd,bhkd->bhqk", q, k) * (Dh ** -0.5)
        p = _softmax(scores, axis=-1)
        o = torch.einsum("bhqk,bhkd->bhqd", p, v)
        return (lambda x, *d: x.permute(*d))(o, 0, 2, 1, 3).reshape(B, Sq, Hq * Dh) @ wo

    a = _A_ref(A_x, A_k_cache, A_v_cache, A_wq, A_wo)
    b = _B_ref(B_x, B_k_cache, B_v_cache, B_wq, B_wo)
    return torch.concatenate([a, b], axis=-1)
```

```cpp
#include <hip/hip_runtime.h>
#include <stdint.h>
#include <type_traits>

#define LAS __attribute__((address_space(3)))
#define DI __device__ __forceinline__
typedef short s16x4 __attribute__((ext_vector_type(4)));
typedef __bf16 bf16x2 __attribute__((ext_vector_type(2)));
typedef __bf16 bf16x8 __attribute__((ext_vector_type(8)));
typedef float f32x2 __attribute__((ext_vector_type(2)));
typedef float f32x4 __attribute__((ext_vector_type(4)));
typedef float f32x16 __attribute__((ext_vector_type(16)));
typedef unsigned u32x2 __attribute__((ext_vector_type(2)));
typedef unsigned u32x4 __attribute__((ext_vector_type(4)));
typedef unsigned u32x8 __attribute__((ext_vector_type(8)));
typedef unsigned u32x12 __attribute__((ext_vector_type(12)));
typedef unsigned u32x16 __attribute__((ext_vector_type(16)));
typedef unsigned u32x32 __attribute__((ext_vector_type(32)));
typedef float f32x8 __attribute__((ext_vector_type(8)));
typedef float f32x32 __attribute__((ext_vector_type(32)));
typedef __amdgpu_buffer_rsrc_t rsrc_t;

#define STAMPS 0
#define KVREC (SK * DH * 2)
#define QTWICE 0
#define PRIOQ1 3
#define DRY 1
#define PRIOQ0 3
#define MAP 0
#define W1ATT 0
#define QPACED 0
#define OPACE 63
#define PRIOT 0
#define ORELEASE 0
#define XBF16 1
#define MINHEADS 4
#define DYNCLAIM 0
#define OWNROLE 0u
#define WOPF 1
#define BAL 0
#define BALB 0u
#define OREAD 0
#define XWAVE 1
#define WROT 0u
#define TWOPOL 0
#define K2ROT 1
#define K2OD 4
#define PRIOO 0
#define POLLSLEEP 2
#define TRACE 0
#define TPER0 0u
#define TPER1 0u
#define PRIO0 0
#define PRIO1 1
#define W1DELAY 0

#define STAMP(k)
constexpr int SQ = 144, DM = 640, NH = 32, SK = 1024, DH = 128;
constexpr float QSCALE = 0.12751743082f;
constexpr float THR = 8.0f;
constexpr unsigned STG = 17408;
constexpr unsigned QXO = 77824;
constexpr unsigned LDS1 = 81920;

struct Params {
  const char* x[2]; const char* k[2]; const char* v[2]; const char* wq[2]; const char* wo[2];
  char* ot; unsigned long long* sync; unsigned short* out; unsigned long long* dbg;
};

DI unsigned cvtpk(float lo, float hi) { f32x2 v = {lo, hi}; bf16x2 b = __builtin_convertvector(v, bf16x2); return __builtin_bit_cast(unsigned, b); }
DI float fmx(float a, float b) { return __builtin_elementwise_maximum(a, b); }
DI float fmx3(float a, float b, float c) { return fmx(fmx(a, b), c); }
DI f32x16 mfma32(bf16x8 a, bf16x8 b, f32x16 c) { return __builtin_amdgcn_mfma_f32_32x32x16_bf16(a, b, c, 0, 0, 0); }
DI f32x4 mfma16(bf16x8 a, bf16x8 b, f32x4 c) { return __builtin_amdgcn_mfma_f32_16x16x32_bf16(a, b, c, 0, 0, 0); }
DI f32x4 mfma16h(s16x4 a, s16x4 b, f32x4 c) { return __builtin_amdgcn_mfma_f32_16x16x16bf16_1k(a, b, c, 0, 0, 0); }
DI bf16x8 lds_rd(unsigned addr) { return *(const LAS bf16x8*)(uintptr_t)addr; }
DI s16x4 lds_tr(unsigned addr) { return __builtin_amdgcn_ds_read_tr16_b64_v4i16((LAS s16x4*)(uintptr_t)addr); }
DI bf16x8 cat(s16x4 a, s16x4 b) { return __builtin_bit_cast(bf16x8, __builtin_shufflevector(a, b, 0, 1, 2, 3, 4, 5, 6, 7)); }
DI bf16x8 pack8(const f32x16& x, int o) { u32x4 w = {cvtpk(x[o], x[o + 1]), cvtpk(x[o + 2], x[o + 3]), cvtpk(x[o + 4], x[o + 5]), cvtpk(x[o + 6], x[o + 7])}; return __builtin_bit_cast(bf16x8, w); }
DI float swap32_max(float x) { auto r = __builtin_amdgcn_permlane32_swap(__float_as_uint(x), __float_as_uint(x), false, false); return fmx(__uint_as_float(r[0]), __uint_as_float(r[1])); }
DI float swap16_max(float x) { auto r = __builtin_amdgcn_permlane16_swap(__float_as_uint(x), __float_as_uint(x), false, false); return fmx(__uint_as_float(r[0]), __uint_as_float(r[1])); }
DI float swap32_sum(float x) { auto r = __builtin_amdgcn_permlane32_swap(__float_as_uint(x), __float_as_uint(x), false, false); return __uint_as_float(r[0]) + __uint_as_float(r[1]); }
DI float swap16_sum(float x) { auto r = __builtin_amdgcn_permlane16_swap(__float_as_uint(x), __float_as_uint(x), false, false); return __uint_as_float(r[0]) + __uint_as_float(r[1]); }
DI unsigned scale_pk(unsigned u, float f) { return cvtpk(__uint_as_float(u << 16) * f, __uint_as_float(u & 0xffff0000u) * f); }
DI bf16x8 scale8(bf16x8 p, float f) { u32x4 w = __builtin_bit_cast(u32x4, p); w[0] = scale_pk(w[0], f); w[1] = scale_pk(w[1], f); w[2] = scale_pk(w[2], f); w[3] = scale_pk(w[3], f); return __builtin_bit_cast(bf16x8, w); }

template <int IMM, bool NT> DI void dma(unsigned m0v, unsigned voff, rsrc_t srd, unsigned soff) {
  if constexpr (NT) asm volatile("s_mov_b32 m0, %0\n\ts_nop 0\n\tbuffer_load_dwordx4 %1, %2, %3 offen offset:%4 nt lds" :: "s"(m0v), "v"(voff), "s"(srd), "s"(soff), "i"(IMM) : "memory");
  else asm volatile("s_mov_b32 m0, %0\n\ts_nop 0\n\tbuffer_load_dwordx4 %1, %2, %3 offen offset:%4 lds" :: "s"(m0v), "v"(voff), "s"(srd), "s"(soff), "i"(IMM) : "memory");
}
template <bool NT> DI void dma_lo16(unsigned m0v, unsigned voff, rsrc_t srd, unsigned soff) {
  if constexpr (NT) asm volatile("s_mov_b32 m0, %0\n\ts_mov_b64 exec, 0xffff\n\tbuffer_load_dwordx4 %1, %2, %3 offen nt lds\n\ts_mov_b64 exec, -1" :: "s"(m0v), "v"(voff), "s"(srd), "s"(soff) : "memory");
  else asm volatile("s_mov_b32 m0, %0\n\ts_mov_b64 exec, 0xffff\n\tbuffer_load_dwordx4 %1, %2, %3 offen lds\n\ts_mov_b64 exec, -1" :: "s"(m0v), "v"(voff), "s"(srd), "s"(soff) : "memory");
}
#define SB() __builtin_amdgcn_sched_barrier(0)
template <int IMM> DI void dma2nt(unsigned m0v, unsigned v0, unsigned v1, rsrc_t srd, unsigned soff) {
  asm volatile("s_mov_b32 m0, %0\n\ts_nop 0\n\tbuffer_load_dwordx4 %1, %3, %4 offen offset:%5 nt lds\n\tbuffer_load_dwordx4 %2, %3, %4 offen offset:%6 nt lds"
               :: "s"(m0v), "v"(v0), "v"(v1), "s"(srd), "s"(soff), "i"(IMM), "i"(IMM + 2048) : "memory");
}
DI float fadd(float a, float b) { float r; asm("v_add_f32_e32 %0, %1, %2" : "=v"(r) : "v"(a), "v"(b)); return r; }
template <class T> DI void pin(T& x) { asm volatile("" : "+v"(x)); }
template <int N> DI void wait_bar() { asm volatile("s_waitcnt vmcnt(%0) lgkmcnt(0)\n\ts_barrier" :: "i"(N) : "memory"); }
DI void lds_bar() { asm volatile("s_waitcnt lgkmcnt(0)\n\ts_barrier" ::: "memory"); }
#define WNT true
#define OAUX 16

template <int P0, int P1> DI void set_prio(unsigned role) {
  asm volatile("s_setprio %1\n\ts_cmp_eq_u32 %0, 0\n\ts_cbranch_scc1 .Lp%=\n\ts_setprio %2\n.Lp%=:" :: "s"(role), "i"(P0), "i"(P1) : "scc");
}
struct KvDma { unsigned kv0, kv1, vv, m0w, sow; rsrc_t srdK, srdV; };

DI unsigned lane_id() { unsigned l; asm volatile("v_mbcnt_lo_u32_b32 %0, -1, 0\n\tv_mbcnt_hi_u32_b32 %0, -1, %0" : "=v"(l)); return l; }
template <int N, int SLEEP = POLLSLEEP> DI bool wait_tags(const unsigned long long* f, unsigned idx, unsigned long long tag) {
#pragma unroll 1
  for (unsigned spin = 0; spin < 200000u; ++spin) {
    bool ok = true;
#pragma unroll
    for (int k = 0; k < N; ++k) ok &= __hip_atomic_load(f + idx + 64 * k, __ATOMIC_RELAXED, __HIP_MEMORY_SCOPE_AGENT) == tag;
    if (__builtin_amdgcn_ballot_w64(!ok) == 0) return true;
    __builtin_amdgcn_s_sleep(SLEEP);
  }
  return false;
}
constexpr unsigned OT_BYTES = 36u * 256u * 1024u;
constexpr unsigned XG_BYTES = 16u * 12288u;
template <int KQ> DI void exchange(const Params& P, const f32x32 (&acc)[3], const unsigned w, const unsigned br, const unsigned i, const unsigned mt, const unsigned nt, const unsigned long long tag) {
  const unsigned lane = lane_id();
  const rsrc_t srdX = __builtin_amdgcn_make_buffer_rsrc((void*)(P.ot + 2u * (size_t)OT_BYTES + (size_t)(br * 30u + i) * XG_BYTES), 0, XG_BYTES, 0x00020000);
  const unsigned xo = 3072u * w + 16u * lane;
#define ACC(e) acc[(e) >> 5][(e) & 31]
#pragma unroll
  for (int v = 0; v < 4; ++v)
    if (v != KQ) {
#pragma unroll
      for (int k = 0; k < 3; ++k) {
        const int e = 24 * v + 8 * k;
        const u32x4 x = {cvtpk(ACC(e), ACC(e + 1)), cvtpk(ACC(e + 2), ACC(e + 3)), cvtpk(ACC(e + 4), ACC(e + 5)), cvtpk(ACC(e + 6), ACC(e + 7))};
        __builtin_amdgcn_raw_buffer_store_b128(x, srdX, xo + (4u * v + KQ) * 12288u + 1024u * k, 0, 16);
      }
    }
  asm volatile("s_waitcnt vmcnt(0)" ::: "memory");
  const unsigned long long* fl = P.sync + 5184u + ((br * 30u + i) * 4u + w) * 4u;
  if (lane == 0) __hip_atomic_store(const_cast<unsigned long long*>(fl) + KQ, tag, __ATOMIC_RELAXED, __HIP_MEMORY_SCOPE_AGENT);
  wait_tags<1, 1>(fl, lane & 3u, tag);
  asm volatile("" ::: "memory");
  STAMP(13);
  u32x4 z[3][3];
#pragma unroll
  for (int v = 0; v < 3; ++v)
#pragma unroll
    for (int k = 0; k < 3; ++k) z[v][k] = __builtin_amdgcn_raw_buffer_load_b128(srdX, xo + (4u * KQ + (v + (v >= KQ))) * 12288u + 1024u * k, 0, 16);
#pragma unroll
  for (int q = 0; q < 6; ++q) {
    const int e = 24 * KQ + 4 * q;
    f32x4 y = {ACC(e), ACC(e + 1), ACC(e + 2), ACC(e + 3)};
#pragma unroll
    for (int v = 0; v < 3; ++v) {
      const unsigned u0 = z[v][q >> 1][2 * (q & 1)], u1 = z[v][q >> 1][2 * (q & 1) + 1];
      y += (f32x4){__uint_as_float(u0 << 16), __uint_as_float(u0 & 0xffff0000u), __uint_as_float(u1 << 16), __uint_as_float(u1 & 0xffff0000u)};
    }
    const unsigned row = 16u * (12u * mt + 6u * (w >> 1) + q) + (lane & 15u);
    const u32x2 o = {cvtpk(y[0], y[1]), cvtpk(y[2], y[3])};
    *(u32x2*)(P.out + (size_t)row * 1280u + 640u * br + 128u * nt + 64u * (w & 1u) + 16u * KQ + 4u * (lane >> 4)) = o;
  }
#undef ACC
}
DI void oproj_phase(const Params& P, const unsigned ldsb, const unsigned w, const unsigned g, const unsigned i, const unsigned long long tag) {
  const unsigned br = g >> 2, kq = g & 3u, mt = (i * 52u) >> 8, nt = i - 5u * mt;
  const rsrc_t srdW = __builtin_amdgcn_make_buffer_rsrc((void*)(P.wo[br] + 256u * nt - 4096), 0, 4096 * 1280 - 256 * nt + 4096, 0x00020000);
  const rsrc_t srdO = __builtin_amdgcn_make_buffer_rsrc((void*)(P.ot + (size_t)br * OT_BYTES + (size_t)mt * (12u * 131072u) - 4096), 0, 12 * 131072 + 4096, 0x00020000);
  f32x32 acc[3];
#pragma unroll
  for (int k = 0; k < 3; ++k) acc[k] = (f32x32){};
  __builtin_amdgcn_s_setprio(PRIOO);
  const unsigned rot = (((0x32100u >> (4u * nt)) & 3u) + K2ROT * mt) & 3u;
  unsigned order = 0;
#pragma unroll
  for (int k = 7; k >= 0; --k) order = (order << 4) | ((k & 4u) | ((k + rot) & 3u));
  unsigned left = 8;
  LAS unsigned* bc = (LAS unsigned*)(uintptr_t)ldsb;
#pragma unroll 1
  while (left) {
    const unsigned lane = lane_id();
    if (w == 0) {
      const unsigned b0 = (12u * mt * 57u) >> 9, b1 = ((12u * mt + 11u) * 57u) >> 9, hd = (order >> (4u * ((lane >> 1) & 7u))) & 7u;
      const unsigned long long* f = P.sync + 64u + br * 256u + 4u * kq + ((lane & 1u) ? b1 : b0) * 32u + (hd >> 2) * 16u + (hd & 3u);
      unsigned n = left;
#pragma unroll 1
      for (unsigned spin = 0; spin < 200000u; ++spin) {
        const unsigned miss = (unsigned)__builtin_amdgcn_ballot_w64(__hip_atomic_load(f, __ATOMIC_RELAXED, __HIP_MEMORY_SCOPE_AGENT) != tag) | (1u << (2u * left));
        const unsigned rdy = (unsigned)__builtin_ctz(miss) >> 1;
        if (rdy == left || rdy >= MINHEADS) { n = rdy; break; }
        __builtin_amdgcn_s_sleep(POLLSLEEP);
      }
      if (lane == 0) *bc = n;
    }
    lds_bar();
    const unsigned hn = __builtin_amdgcn_readfirstlane(*(volatile LAS unsigned*)bc), hl = order;
    left -= hn; order = hn == 8u ? 0u : order >> (4u * hn);
    const unsigned hh = lane >> 5, prow = (lane >> 2) & 7u, ppos = lane & 3u, tq = (lane >> 2) & 3u, g4 = lane >> 4;
    u32x16 od = lane;
    od[0] = 3u * w * 131072u + 16u * lane + 4096u; od[1] = od[0] + 131072u - 1024u; od[2] = od[0] + 262144u - 2048u;
    od[3] = (8u * w + prow) * 1280u + 64u * hh + 16u * (ppos ^ (2u * (prow >> 2))) + 3072u; od[4] = od[3] + 128u - 1024u;
    od[5] = ldsb + 10240u * (w >> 1) + 16u * lane; od[6] = ldsb + 3072u + 1024u * (w & 1u) + 5120u * hh + 288u * (g4 & 1u) + 64u * tq + 8u * ppos; od[7] = od[6] ^ 32u;
    od[8] = od[5] + 40960u; od[9] = od[6] + 40960u; od[10] = od[7] + 40960u;
    lds_bar();
    asm volatile(
    "s_mov_b32 s67, %[hl]\n"
    "s_sub_u32 s66, %[hn], 1\n"
    "s_and_b32 s68, s67, 15\n"
    "s_lshr_b32 s67, s67, 4\n"
    "s_lshl_b32 s64, s68, 12\n"
    "s_add_u32 s64, s64, %[obeg]\n"
    "s_mul_i32 s65, s68, 163840\n"
    "s_add_u32 s65, s65, %[wbeg]\n"
    "s_add_u32 m0, %[m0o], 2048\n"
    "s_nop 0\n"
    "buffer_load_dwordx4 v179, %[srdW], s65 offen offset:1024 lds\n"
    "buffer_load_dwordx4 v180, %[srdW], s65 offen offset:2048 lds\n"
    "s_add_u32 m0, %[m0o], 0\n"
    "s_nop 0\n"
    "buffer_load_dwordx4 v176, %[srdO], s64 offen offset:0 sc1 lds\n"
    "buffer_load_dwordx4 v177, %[srdO], s64 offen offset:1024 sc1 lds\n"
    "buffer_load_dwordx4 v178, %[srdO], s64 offen offset:2048 sc1 lds\n"
    "s_add_u32 s65, s65, 40960\n"
    "s_add_u32 s64, s64, 1024\n"
    "v_mov_b32 v172, 0\n"
    "v_mov_b32 v173, 0\n"
    "v_mov_b32 v174, 0\n"
    "v_mov_b32 v175, 0\n"
    "v_mov_b32 v148, 0\n"
    "v_mov_b32 v149, 0\n"
    "v_mov_b32 v150, 0\n"
    "v_mov_b32 v151, 0\n"
    "v_mov_b32 v152, 0\n"
    "v_mov_b32 v153, 0\n"
    "v_mov_b32 v154, 0\n"
    "v_mov_b32 v155, 0\n"
    "v_mov_b32 v156, 0\n"
    "v_mov_b32 v157, 0\n"
    "v_mov_b32 v158, 0\n"
    "v_mov_b32 v159, 0\n"
    "s_add_u32 m0, %[m0o], 22528\n"
    "s_nop 0\n"
    "buffer_load_dwordx4 v179, %[srdW], s65 offen offset:1024 lds\n"
    "buffer_load_dwordx4 v180, %[srdW], s65 offen offset:2048 lds\n"
    "s_add_u32 m0, %[m0o], 20480\n"
    "s_nop 0\n"
    "buffer_load_dwordx4 v176, %[srdO], s64 offen offset:0 sc1 lds\n"
    "buffer_load_dwordx4 v177, %[srdO], s64 offen offset:1024 sc1 lds\n"
    "buffer_load_dwordx4 v178, %[srdO], s64 offen offset:2048 sc1 lds\n"
    "s_add_u32 s65, s65, 40960\n"
    "s_add_u32 s64, s64, 1024\n"
    "s_add_u32 m0, %[m0o], 43008\n"
    "s_nop 0\n"
    "buffer_load_dwordx4 v179, %[srdW], s65 offen offset:1024 lds\n"
    "buffer_load_dwordx4 v180, %[srdW], s65 offen offset:2048 lds\n"
    "s_add_u32 m0, %[m0o], 40960\n"
    "s_nop 0\n"
    "buffer_load_dwordx4 v176, %[srdO], s64 offen offset:0 sc1 lds\n"
    "buffer_load_dwordx4 v177, %[srdO], s64 offen offset:1024 sc1 lds\n"
    "buffer_load_dwordx4 v178, %[srdO], s64 offen offset:2048 sc1 lds\n"
    "s_add_u32 s65, s65, 40960\n"
    "s_add_u32 s64, s64, 1024\n"
    "s_add_u32 m0, %[m0o], 63488\n"
    "s_nop 0\n"
    "buffer_load_dwordx4 v179, %[srdW], s65 offen offset:1024 lds\n"
    "buffer_load_dwordx4 v180, %[srdW], s65 offen offset:2048 lds\n"
    "s_add_u32 m0, %[m0o], 61440\n"
    "s_nop 0\n"
    "buffer_load_dwordx4 v176, %[srdO], s64 offen offset:0 sc1 lds\n"
    "buffer_load_dwordx4 v177, %[srdO], s64 offen offset:1024 sc1 lds\n"
    "buffer_load_dwordx4 v178, %[srdO], s64 offen offset:2048 sc1 lds\n"
    "s_add_u32 s65, s65, 40960\n"
    "s_add_u32 s64, s64, 1024\n"
    "s_waitcnt vmcnt(15) lgkmcnt(0)\n"
    "s_barrier\n"
    "ds_read_b64_tr_b16 v[120:121], v182 offset:0\n"
    "ds_read_b64_tr_b16 v[122:123], v182 offset:10240\n"
    "ds_read_b128 v[96:99], v181 offset:0\n"
    "ds_read_b128 v[100:103], v181 offset:1024\n"
    "ds_read_b64_tr_b16 v[124:125], v183 offset:0\n"
    "ds_read_b64_tr_b16 v[126:127], v183 offset:10240\n"
    "ds_read_b128 v[104:107], v181 offset:2048\n"
    "ds_read_b64_tr_b16 v[128:129], v182 offset:512\n"
    "ds_read_b64_tr_b16 v[130:131], v182 offset:10752\n"
    "ds_read_b128 v[108:111], v181 offset:5120\n"
    "ds_read_b128 v[112:115], v181 offset:6144\n"
    "ds_read_b128 v[116:119], v181 offset:7168\n"
    "ds_read_b64_tr_b16 v[132:133], v183 offset:512\n"
    "ds_read_b64_tr_b16 v[134:135], v183 offset:10752\n"
    "s_cmp_eq_u32 s66, 0\n"
    "s_cbranch_scc1 .Lol_%=\n"
    ".Lo_%=:\n"
    "s_and_b32 s68, s67, 15\n"
    "s_lshr_b32 s67, s67, 4\n"
    "s_lshl_b32 s64, s68, 12\n"
    "s_add_u32 s64, s64, %[obeg]\n"
    "s_mul_i32 s65, s68, 163840\n"
    "s_add_u32 s65, s65, %[wbeg]\n"
    "s_waitcnt vmcnt(10) lgkmcnt(0)\n"
    "s_barrier\n"
    "s_add_u32 m0, %[m0o], 2048\n"
    "v_mfma_f32_16x16x32_bf16 v[84:87], v[172:175], v[148:151], v[84:87]\n"
    "ds_read_b64_tr_b16 v[160:161], v182 offset:20480\n"
    "v_mfma_f32_16x16x32_bf16 v[88:91], v[172:175], v[152:155], v[88:91]\n"
    "buffer_load_dwordx4 v179, %[srdW], s65 offen offset:1024 lds\n"
    "v_mfma_f32_16x16x32_bf16 v[92:95], v[172:175], v[156:159], v[92:95]\n"
    "ds_read_b64_tr_b16 v[162:163], v182 offset:30720\n"
    "v_mfma_f32_16x16x32_bf16 v[0:3], v[120:123], v[96:99], v[0:3]\n"
    "ds_read_b128 v[136:139], v181 offset:20480\n"
    "v_mfma_f32_16x16x32_bf16 v[4:7], v[120:123], v[100:103], v[4:7]\n"
    "ds_read_b128 v[140:143], v181 offset:21504\n"
    "v_mfma_f32_16x16x32_bf16 v[8:11], v[120:123], v[104:107], v[8:11]\n"
    "ds_read_b64_tr_b16 v[164:165], v183 offset:20480\n"
    "v_mfma_f32_16x16x32_bf16 v[12:15], v[120:123], v[108:111], v[12:15]\n"
    "buffer_load_dwordx4 v180, %[srdW], s65 offen offset:2048 lds\n"
    "s_add_u32 m0, %[m0o], 0\n"
    "v_mfma_f32_16x16x32_bf16 v[16:19], v[120:123], v[112:115], v[16:19]\n"
    "ds_read_b64_tr_b16 v[166:167], v183 offset:30720\n"
    "v_mfma_f32_16x16x32_bf16 v[20:23], v[120:123], v[116:119], v[20:23]\n"
    "ds_read_b128 v[144:147], v181 offset:22528\n"
    "v_mfma_f32_16x16x32_bf16 v[24:27], v[124:127], v[96:99], v[24:27]\n"
    "ds_read_b64_tr_b16 v[168:169], v182 offset:20992\n"
    "v_mfma_f32_16x16x32_bf16 v[28:31], v[124:127], v[100:103], v[28:31]\n"
    "ds_read_b64_tr_b16 v[170:171], v182 offset:31232\n"
    "v_mfma_f32_16x16x32_bf16 v[32:35], v[124:127], v[104:107], v[32:35]\n"
    "buffer_load_dwordx4 v176, %[srdO], s64 offen offset:0 sc1 lds\n"
    "s_add_u32 s65, s65, 40960\n"
    "v_mfma_f32_16x16x32_bf16 v[36:39], v[124:127], v[108:111], v[36:39]\n"
    "ds_read_b128 v[148:151], v181 offset:25600\n"
    "v_mfma_f32_16x16x32_bf16 v[40:43], v[124:127], v[112:115], v[40:43]\n"
    "ds_read_b128 v[152:155], v181 offset:26624\n"
    "v_mfma_f32_16x16x32_bf16 v[44:47], v[124:127], v[116:119], v[44:47]\n"
    "ds_read_b128 v[156:159], v181 offset:27648\n"
    "v_mfma_f32_16x16x32_bf16 v[48:51], v[128:131], v[96:99], v[48:51]\n"
    "buffer_load_dwordx4 v177, %[srdO], s64 offen offset:1024 sc1 lds\n"
    "v_mfma_f32_16x16x32_bf16 v[52:55], v[128:131], v[100:103], v[52:55]\n"
    "ds_read_b64_tr_b16 v[172:173], v183 offset:20992\n"
    "v_mfma_f32_16x16x32_bf16 v[56:59], v[128:131], v[104:107], v[56:59]\n"
    "ds_read_b64_tr_b16 v[174:175], v183 offset:31232\n"
    "v_mfma_f32_16x16x32_bf16 v[60:63], v[128:131], v[108:111], v[60:63]\n"
    "v_mfma_f32_16x16x32_bf16 v[64:67], v[128:131], v[112:115], v[64:67]\n"
    "buffer_load_dwordx4 v178, %[srdO], s64 offen offset:2048 sc1 lds\n"
    "s_add_u32 s64, s64, 1024\n"
    "v_mfma_f32_16x16x32_bf16 v[68:71], v[128:131], v[116:119], v[68:71]\n"
    "v_mfma_f32_16x16x32_bf16 v[72:75], v[132:135], v[96:99], v[72:75]\n"
    "v_mfma_f32_16x16x32_bf16 v[76:79], v[132:135], v[100:103], v[76:79]\n"
    "v_mfma_f32_16x16x32_bf16 v[80:83], v[132:135], v[104:107], v[80:83]\n"
    "s_waitcnt vmcnt(10) lgkmcnt(0)\n"
    "s_barrier\n"
    "s_add_u32 m0, %[m0o], 22528\n"
    "v_mfma_f32_16x16x32_bf16 v[84:87], v[132:135], v[108:111], v[84:87]\n"
    "ds_read_b64_tr_b16 v[120:121], v185 offset:0\n"
    "v_mfma_f32_16x16x32_bf16 v[88:91], v[132:135], v[112:115], v[88:91]\n"
    "buffer_load_dwordx4 v179, %[srdW], s65 offen offset:1024 lds\n"
    "v_mfma_f32_16x16x32_bf16 v[92:95], v[132:135], v[116:119], v[92:95]\n"
    "ds_read_b64_tr_b16 v[122:123], v185 offset:10240\n"
    "v_mfma_f32_16x16x32_bf16 v[0:3], v[160:163], v[136:139], v[0:3]\n"
    "ds_read_b128 v[96:99], v184 offset:0\n"
    "v_mfma_f32_16x16x32_bf16 v[4:7], v[160:163], v[140:143], v[4:7]\n"
    "ds_read_b128 v[100:103], v184 offset:1024\n"
    "v_mfma_f32_16x16x32_bf16 v[8:11], v[160:163], v[144:147], v[8:11]\n"
    "ds_read_b64_tr_b16 v[124:125], v186 offset:0\n"
    "v_mfma_f32_16x16x32_bf16 v[12:15], v[160:163], v[148:151], v[12:15]\n"
    "buffer_load_dwordx4 v180, %[srdW], s65 offen offset:2048 lds\n"
    "s_add_u32 m0, %[m0o], 20480\n"
    "v_mfma_f32_16x16x32_bf16 v[16:19], v[160:163], v[152:155], v[16:19]\n"
    "ds_read_b64_tr_b16 v[126:127], v186 offset:10240\n"
    "v_mfma_f32_16x16x32_bf16 v[20:23], v[160:163], v[156:159], v[20:23]\n"
    "ds_read_b128 v[104:107], v184 offset:2048\n"
    "v_mfma_f32_16x16x32_bf16 v[24:27], v[164:167], v[136:139], v[24:27]\n"
    "ds_read_b64_tr_b16 v[128:129], v185 offset:512\n"
    "v_mfma_f32_16x16x32_bf16 v[28:31], v[164:167], v[140:143], v[28:31]\n"
    "ds_read_b64_tr_b16 v[130:131], v185 offset:10752\n"
    "v_mfma_f32_16x16x32_bf16 v[32:35], v[164:167], v[144:147], v[32:35]\n"
    "buffer_load_dwordx4 v176, %[srdO], s64 offen offset:0 sc1 lds\n"
    "s_add_u32 s65, s65, 40960\n"
    "v_mfma_f32_16x16x32_bf16 v[36:39], v[164:167], v[148:151], v[36:39]\n"
    "ds_read_b128 v[108:111], v184 offset:5120\n"
    "v_mfma_f32_16x16x32_bf16 v[40:43], v[164:167], v[152:155], v[40:43]\n"
    "ds_read_b128 v[112:115], v184 offset:6144\n"
    "v_mfma_f32_16x16x32_bf16 v[44:47], v[164:167], v[156:159], v[44:47]\n"
    "ds_read_b128 v[116:119], v184 offset:7168\n"
    "v_mfma_f32_16x16x32_bf16 v[48:51], v[168:171], v[136:139], v[48:51]\n"
    "buffer_load_dwordx4 v177, %[srdO], s64 offen offset:1024 sc1 lds\n"
    "v_mfma_f32_16x16x32_bf16 v[52:55], v[168:171], v[140:143], v[52:55]\n"
    "ds_read_b64_tr_b16 v[132:133], v186 offset:512\n"
    "v_mfma_f32_16x16x32_bf16 v[56:59], v[168:171], v[144:147], v[56:59]\n"
    "ds_read_b64_tr_b16 v[134:135], v186 offset:10752\n"
    "v_mfma_f32_16x16x32_bf16 v[60:63], v[168:171], v[148:151], v[60:63]\n"
    "v_mfma_f32_16x16x32_bf16 v[64:67], v[168:171], v[152:155], v[64:67]\n"
    "buffer_load_dwordx4 v178, %[srdO], s64 offen offset:2048 sc1 lds\n"
    "s_add_u32 s64, s64, 1024\n"
    "v_mfma_f32_16x16x32_bf16 v[68:71], v[168:171], v[156:159], v[68:71]\n"
    "v_mfma_f32_16x16x32_bf16 v[72:75], v[172:175], v[136:139], v[72:75]\n"
    "v_mfma_f32_16x16x32_bf16 v[76:79], v[172:175], v[140:143], v[76:79]\n"
    "v_mfma_f32_16x16x32_bf16 v[80:83], v[172:175], v[144:147], v[80:83]\n"
    "s_waitcnt vmcnt(10) lgkmcnt(0)\n"
    "s_barrier\n"
    "s_add_u32 m0, %[m0o], 43008\n"
    "v_mfma_f32_16x16x32_bf16 v[84:87], v[172:175], v[148:151], v[84:87]\n"
    "ds_read_b64_tr_b16 v[160:161], v185 offset:20480\n"
    "v_mfma_f32_16x16x32_bf16 v[88:91], v[172:175], v[152:155], v[88:91]\n"
    "buffer_load_dwordx4 v179, %[srdW], s65 offen offset:1024 lds\n"
    "v_mfma_f32_16x16x32_bf16 v[92:95], v[172:175], v[156:159], v[92:95]\n"
    "ds_read_b64_tr_b16 v[162:163], v185 offset:30720\n"
    "v_mfma_f32_16x16x32_bf16 v[0:3], v[120:123], v[96:99], v[0:3]\n"
    "ds_read_b128 v[136:139], v184 offset:20480\n"
    "v_mfma_f32_16x16x32_bf16 v[4:7], v[120:123], v[100:103], v[4:7]\n"
    "ds_read_b128 v[140:143], v184 offset:21504\n"
    "v_mfma_f32_16x16x32_bf16 v[8:11], v[120:123], v[104:107], v[8:11]\n"
    "ds_read_b64_tr_b16 v[164:165], v186 offset:20480\n"
    "v_mfma_f32_16x16x32_bf16 v[12:15], v[120:123], v[108:111], v[12:15]\n"
    "buffer_load_dwordx4 v180, %[srdW], s65 offen offset:2048 lds\n"
    "s_add_u32 m0, %[m0o], 40960\n"
    "v_mfma_f32_16x16x32_bf16 v[16:19], v[120:123], v[112:115], v[16:19]\n"
    "ds_read_b64_tr_b16 v[166:167], v186 offset:30720\n"
    "v_mfma_f32_16x16x32_bf16 v[20:23], v[120:123], v[116:119], v[20:23]\n"
    "ds_read_b128 v[144:147], v184 offset:22528\n"
    "v_mfma_f32_16x16x32_bf16 v[24:27], v[124:127], v[96:99], v[24:27]\n"
    "ds_read_b64_tr_b16 v[168:169], v185 offset:20992\n"
    "v_mfma_f32_16x16x32_bf16 v[28:31], v[124:127], v[100:103], v[28:31]\n"
    "ds_read_b64_tr_b16 v[170:171], v185 offset:31232\n"
    "v_mfma_f32_16x16x32_bf16 v[32:35], v[124:127], v[104:107], v[32:35]\n"
    "buffer_load_dwordx4 v176, %[srdO], s64 offen offset:0 sc1 lds\n"
    "s_add_u32 s65, s65, 40960\n"
    "v_mfma_f32_16x16x32_bf16 v[36:39], v[124:127], v[108:111], v[36:39]\n"
    "ds_read_b128 v[148:151], v184 offset:25600\n"
    "v_mfma_f32_16x16x32_bf16 v[40:43], v[124:127], v[112:115], v[40:43]\n"
    "ds_read_b128 v[152:155], v184 offset:26624\n"
    "v_mfma_f32_16x16x32_bf16 v[44:47], v[124:127], v[116:119], v[44:47]\n"
    "ds_read_b128 v[156:159], v184 offset:27648\n"
    "v_mfma_f32_16x16x32_bf16 v[48:51], v[128:131], v[96:99], v[48:51]\n"
    "buffer_load_dwordx4 v177, %[srdO], s64 offen offset:1024 sc1 lds\n"
    "v_mfma_f32_16x16x32_bf16 v[52:55], v[128:131], v[100:103], v[52:55]\n"
    "ds_read_b64_tr_b16 v[172:173], v186 offset:20992\n"
    "v_mfma_f32_16x16x32_bf16 v[56:59], v[128:131], v[104:107], v[56:59]\n"
    "ds_read_b64_tr_b16 v[174:175], v186 offset:31232\n"
    "v_mfma_f32_16x16x32_bf16 v[60:63], v[128:131], v[108:111], v[60:63]\n"
    "v_mfma_f32_16x16x32_bf16 v[64:67], v[128:131], v[112:115], v[64:67]\n"
    "buffer_load_dwordx4 v178, %[srdO], s64 offen offset:2048 sc1 lds\n"
    "s_add_u32 s64, s64, 1024\n"
    "v_mfma_f32_16x16x32_bf16 v[68:71], v[128:131], v[116:119], v[68:71]\n"
    "v_mfma_f32_16x16x32_bf16 v[72:75], v[132:135], v[96:99], v[72:75]\n"
    "v_mfma_f32_16x16x32_bf16 v[76:79], v[132:135], v[100:103], v[76:79]\n"
    "v_mfma_f32_16x16x32_bf16 v[80:83], v[132:135], v[104:107], v[80:83]\n"
    "s_waitcnt vmcnt(10) lgkmcnt(0)\n"
    "s_barrier\n"
    "s_add_u32 m0, %[m0o], 63488\n"
    "v_mfma_f32_16x16x32_bf16 v[84:87], v[132:135], v[108:111], v[84:87]\n"
    "ds_read_b64_tr_b16 v[120:121], v182 offset:0\n"
    "v_mfma_f32_16x16x32_bf16 v[88:91], v[132:135], v[112:115], v[88:91]\n"
    "buffer_load_dwordx4 v179, %[srdW], s65 offen offset:1024 lds\n"
    "v_mfma_f32_16x16x32_bf16 v[92:95], v[132:135], v[116:119], v[92:95]\n"
    "ds_read_b64_tr_b16 v[122:123], v182 offset:10240\n"
    "v_mfma_f32_16x16x32_bf16 v[0:3], v[160:163], v[136:139], v[0:3]\n"
    "ds_read_b128 v[96:99], v181 offset:0\n"
    "v_mfma_f32_16x16x32_bf16 v[4:7], v[160:163], v[140:143], v[4:7]\n"
    "ds_read_b128 v[100:103], v181 offset:1024\n"
    "v_mfma_f32_16x16x32_bf16 v[8:11], v[160:163], v[144:147], v[8:11]\n"
    "ds_read_b64_tr_b16 v[124:125], v183 offset:0\n"
    "v_mfma_f32_16x16x32_bf16 v[12:15], v[160:163], v[148:151], v[12:15]\n"
    "buffer_load_dwordx4 v180, %[srdW], s65 offen offset:2048 lds\n"
    "s_add_u32 m0, %[m0o], 61440\n"
    "v_mfma_f32_16x16x32_bf16 v[16:19], v[160:163], v[152:155], v[16:19]\n"
    "ds_read_b64_tr_b16 v[126:127], v183 offset:10240\n"
    "v_mfma_f32_16x16x32_bf16 v[20:23], v[160:163], v[156:159], v[20:23]\n"
    "ds_read_b128 v[104:107], v181 offset:2048\n"
    "v_mfma_f32_16x16x32_bf16 v[24:27], v[164:167], v[136:139], v[24:27]\n"
    "ds_read_b64_tr_b16 v[128:129], v182 offset:512\n"
    "v_mfma_f32_16x16x32_bf16 v[28:31], v[164:167], v[140:143], v[28:31]\n"
    "ds_read_b64_tr_b16 v[130:131], v182 offset:10752\n"
    "v_mfma_f32_16x16x32_bf16 v[32:35], v[164:167], v[144:147], v[32:35]\n"
    "buffer_load_dwordx4 v176, %[srdO], s64 offen offset:0 sc1 lds\n"
    "s_add_u32 s65, s65, 40960\n"
    "v_mfma_f32_16x16x32_bf16 v[36:39], v[164:167], v[148:151], v[36:39]\n"
    "ds_read_b128 v[108:111], v181 offset:5120\n"
    "v_mfma_f32_16x16x32_bf16 v[40:43], v[164:167], v[152:155], v[40:43]\n"
    "ds_read_b128 v[112:115], v181 offset:6144\n"
    "v_mfma_f32_16x16x32_bf16 v[44:47], v[164:167], v[156:159], v[44:47]\n"
    "ds_read_b128 v[116:119], v181 offset:7168\n"
    "v_mfma_f32_16x16x32_bf16 v[48:51], v[168:171], v[136:139], v[48:51]\n"
    "buffer_load_dwordx4 v177, %[srdO], s64 offen offset:1024 sc1 lds\n"
    "v_mfma_f32_16x16x32_bf16 v[52:55], v[168:171], v[140:143], v[52:55]\n"
    "ds_read_b64_tr_b16 v[132:133], v183 offset:512\n"
    "v_mfma_f32_16x16x32_bf16 v[56:59], v[168:171], v[144:147], v[56:59]\n"
    "ds_read_b64_tr_b16 v[134:135], v183 offset:10752\n"
    "v_mfma_f32_16x16x32_bf16 v[60:63], v[168:171], v[148:151], v[60:63]\n"
    "v_mfma_f32_16x16x32_bf16 v[64:67], v[168:171], v[152:155], v[64:67]\n"
    "buffer_load_dwordx4 v178, %[srdO], s64 offen offset:2048 sc1 lds\n"
    "s_add_u32 s64, s64, 1024\n"
    "v_mfma_f32_16x16x32_bf16 v[68:71], v[168:171], v[156:159], v[68:71]\n"
    "v_mfma_f32_16x16x32_bf16 v[72:75], v[172:175], v[136:139], v[72:75]\n"
    "v_mfma_f32_16x16x32_bf16 v[76:79], v[172:175], v[140:143], v[76:79]\n"
    "v_mfma_f32_16x16x32_bf16 v[80:83], v[172:175], v[144:147], v[80:83]\n"
    "s_sub_u32 s66, s66, 1\n"
    "s_cmp_lg_u32 s66, 0\n"
    "s_cbranch_scc1 .Lo_%=\n"
    ".Lol_%=:\n"
    "s_waitcnt vmcnt(10) lgkmcnt(0)\n"
    "s_barrier\n"
    "v_mfma_f32_16x16x32_bf16 v[84:87], v[172:175], v[148:151], v[84:87]\n"
    "ds_read_b64_tr_b16 v[160:161], v182 offset:20480\n"
    "v_mfma_f32_16x16x32_bf16 v[88:91], v[172:175], v[152:155], v[88:91]\n"
    "v_mfma_f32_16x16x32_bf16 v[92:95], v[172:175], v[156:159], v[92:95]\n"
    "ds_read_b64_tr_b16 v[162:163], v182 offset:30720\n"
    "v_mfma_f32_16x16x32_bf16 v[0:3], v[120:123], v[96:99], v[0:3]\n"
    "ds_read_b128 v[136:139], v181 offset:20480\n"
    "v_mfma_f32_16x16x32_bf16 v[4:7], v[120:123], v[100:103], v[4:7]\n"
    "ds_read_b128 v[140:143], v181 offset:21504\n"
    "v_mfma_f32_16x16x32_bf16 v[8:11], v[120:123], v[104:107], v[8:11]\n"
    "ds_read_b64_tr_b16 v[164:165], v183 offset:20480\n"
    "v_mfma_f32_16x16x32_bf16 v[12:15], v[120:123], v[108:111], v[12:15]\n"
    "v_mfma_f32_16x16x32_bf16 v[16:19], v[120:123], v[112:115], v[16:19]\n"
    "ds_read_b64_tr_b16 v[166:167], v183 offset:30720\n"
    "v_mfma_f32_16x16x32_bf16 v[20:23], v[120:123], v[116:119], v[20:23]\n"
    "ds_read_b128 v[144:147], v181 offset:22528\n"
    "v_mfma_f32_16x16x32_bf16 v[24:27], v[124:127], v[96:99], v[24:27]\n"
    "ds_read_b64_tr_b16 v[168:169], v182 offset:20992\n"
    "v_mfma_f32_16x16x32_bf16 v[28:31], v[124:127], v[100:103], v[28:31]\n"
    "ds_read_b64_tr_b16 v[170:171], v182 offset:31232\n"
    "v_mfma_f32_16x16x32_bf16 v[32:35], v[124:127], v[104:107], v[32:35]\n"
    "v_mfma_f32_16x16x32_bf16 v[36:39], v[124:127], v[108:111], v[36:39]\n"
    "ds_read_b128 v[148:151], v181 offset:25600\n"
    "v_mfma_f32_16x16x32_bf16 v[40:43], v[124:127], v[112:115], v[40:43]\n"
    "ds_read_b128 v[152:155], v181 offset:26624\n"
    "v_mfma_f32_16x16x32_bf16 v[44:47], v[124:127], v[116:119], v[44:47]\n"
    "ds_read_b128 v[156:159], v181 offset:27648\n"
    "v_mfma_f32_16x16x32_bf16 v[48:51], v[128:131], v[96:99], v[48:51]\n"
    "v_mfma_f32_16x16x32_bf16 v[52:55], v[128:131], v[100:103], v[52:55]\n"
    "ds_read_b64_tr_b16 v[172:173], v183 offset:20992\n"
    "v_mfma_f32_16x16x32_bf16 v[56:59], v[128:131], v[104:107], v[56:59]\n"
    "ds_read_b64_tr_b16 v[174:175], v183 offset:31232\n"
    "v_mfma_f32_16x16x32_bf16 v[60:63], v[128:131], v[108:111], v[60:63]\n"
    "v_mfma_f32_16x16x32_bf16 v[64:67], v[128:131], v[112:115], v[64:67]\n"
    "v_mfma_f32_16x16x32_bf16 v[68:71], v[128:131], v[116:119], v[68:71]\n"
    "v_mfma_f32_16x16x32_bf16 v[72:75], v[132:135], v[96:99], v[72:75]\n"
    "v_mfma_f32_16x16x32_bf16 v[76:79], v[132:135], v[100:103], v[76:79]\n"
    "v_mfma_f32_16x16x32_bf16 v[80:83], v[132:135], v[104:107], v[80:83]\n"
    "s_waitcnt vmcnt(5) lgkmcnt(0)\n"
    "s_barrier\n"
    "v_mfma_f32_16x16x32_bf16 v[84:87], v[132:135], v[108:111], v[84:87]\n"
    "ds_read_b64_tr_b16 v[120:121], v185 offset:0\n"
    "v_mfma_f32_16x16x32_bf16 v[88:91], v[132:135], v[112:115], v[88:91]\n"
    "v_mfma_f32_16x16x32_bf16 v[92:95], v[132:135], v[116:119], v[92:95]\n"
    "ds_read_b64_tr_b16 v[122:123], v185 offset:10240\n"
    "v_mfma_f32_16x16x32_bf16 v[0:3], v[160:163], v[136:139], v[0:3]\n"
    "ds_read_b128 v[96:99], v184 offset:0\n"
    "v_mfma_f32_16x16x32_bf16 v[4:7], v[160:163], v[140:143], v[4:7]\n"
    "ds_read_b128 v[100:103], v184 offset:1024\n"
    "v_mfma_f32_16x16x32_bf16 v[8:11], v[160:163], v[144:147], v[8:11]\n"
    "ds_read_b64_tr_b16 v[124:125], v186 offset:0\n"
    "v_mfma_f32_16x16x32_bf16 v[12:15], v[160:163], v[148:151], v[12:15]\n"
    "v_mfma_f32_16x16x32_bf16 v[16:19], v[160:163], v[152:155], v[16:19]\n"
    "ds_read_b64_tr_b16 v[126:127], v186 offset:10240\n"
    "v_mfma_f32_16x16x32_bf16 v[20:23], v[160:163], v[156:159], v[20:23]\n"
    "ds_read_b128 v[104:107], v184 offset:2048\n"
    "v_mfma_f32_16x16x32_bf16 v[24:27], v[164:167], v[136:139], v[24:27]\n"
    "ds_read_b64_tr_b16 v[128:129], v185 offset:512\n"
    "v_mfma_f32_16x16x32_bf16 v[28:31], v[164:167], v[140:143], v[28:31]\n"
    "ds_read_b64_tr_b16 v[130:131], v185 offset:10752\n"
    "v_mfma_f32_16x16x32_bf16 v[32:35], v[164:167], v[144:147], v[32:35]\n"
    "v_mfma_f32_16x16x32_bf16 v[36:39], v[164:167], v[148:151], v[36:39]\n"
    "ds_read_b128 v[108:111], v184 offset:5120\n"
    "v_mfma_f32_16x16x32_bf16 v[40:43], v[164:167], v[152:155], v[40:43]\n"
    "ds_read_b128 v[112:115], v184 offset:6144\n"
    "v_mfma_f32_16x16x32_bf16 v[44:47], v[164:167], v[156:159], v[44:47]\n"
    "ds_read_b128 v[116:119], v184 offset:7168\n"
    "v_mfma_f32_16x16x32_bf16 v[48:51], v[168:171], v[136:139], v[48:51]\n"
    "v_mfma_f32_16x16x32_bf16 v[52:55], v[168:171], v[140:143], v[52:55]\n"
    "ds_read_b64_tr_b16 v[132:133], v186 offset:512\n"
    "v_mfma_f32_16x16x32_bf16 v[56:59], v[168:171], v[144:147], v[56:59]\n"
    "ds_read_b64_tr_b16 v[134:135], v186 offset:10752\n"
    "v_mfma_f32_16x16x32_bf16 v[60:63], v[168:171], v[148:151], v[60:63]\n"
    "v_mfma_f32_16x16x32_bf16 v[64:67], v[168:171], v[152:155], v[64:67]\n"
    "v_mfma_f32_16x16x32_bf16 v[68:71], v[168:171], v[156:159], v[68:71]\n"
    "v_mfma_f32_16x16x32_bf16 v[72:75], v[172:175], v[136:139], v[72:75]\n"
    "v_mfma_f32_16x16x32_bf16 v[76:79], v[172:175], v[140:143], v[76:79]\n"
    "v_mfma_f32_16x16x32_bf16 v[80:83], v[172:175], v[144:147], v[80:83]\n"
    "s_waitcnt vmcnt(0) lgkmcnt(0)\n"
    "s_barrier\n"
    "v_mfma_f32_16x16x32_bf16 v[84:87], v[172:175], v[148:151], v[84:87]\n"
    "ds_read_b64_tr_b16 v[160:161], v185 offset:20480\n"
    "v_mfma_f32_16x16x32_bf16 v[88:91], v[172:175], v[152:155], v[88:91]\n"
    "v_mfma_f32_16x16x32_bf16 v[92:95], v[172:175], v[156:159], v[92:95]\n"
    "ds_read_b64_tr_b16 v[162:163], v185 offset:30720\n"
    "v_mfma_f32_16x16x32_bf16 v[0:3], v[120:123], v[96:99], v[0:3]\n"
    "ds_read_b128 v[136:139], v184 offset:20480\n"
    "v_mfma_f32_16x16x32_bf16 v[4:7], v[120:123], v[100:103], v[4:7]\n"
    "ds_read_b128 v[140:143], v184 offset:21504\n"
    "v_mfma_f32_16x16x32_bf16 v[8:11], v[120:123], v[104:107], v[8:11]\n"
    "ds_read_b64_tr_b16 v[164:165], v186 offset:20480\n"
    "v_mfma_f32_16x16x32_bf16 v[12:15], v[120:123], v[108:111], v[12:15]\n"
    "v_mfma_f32_16x16x32_bf16 v[16:19], v[120:123], v[112:115], v[16:19]\n"
    "ds_read_b64_tr_b16 v[166:167], v186 offset:30720\n"
    "v_mfma_f32_16x16x32_bf16 v[20:23], v[120:123], v[116:119], v[20:23]\n"
    "ds_read_b128 v[144:147], v184 offset:22528\n"
    "v_mfma_f32_16x16x32_bf16 v[24:27], v[124:127], v[96:99], v[24:27]\n"
    "ds_read_b64_tr_b16 v[168:169], v185 offset:20992\n"
    "v_mfma_f32_16x16x32_bf16 v[28:31], v[124:127], v[100:103], v[28:31]\n"
    "ds_read_b64_tr_b16 v[170:171], v185 offset:31232\n"
    "v_mfma_f32_16x16x32_bf16 v[32:35], v[124:127], v[104:107], v[32:35]\n"
    "v_mfma_f32_16x16x32_bf16 v[36:39], v[124:127], v[108:111], v[36:39]\n"
    "ds_read_b128 v[148:151], v184 offset:25600\n"
    "v_mfma_f32_16x16x32_bf16 v[40:43], v[124:127], v[112:115], v[40:43]\n"
    "ds_read_b128 v[152:155], v184 offset:26624\n"
    "v_mfma_f32_16x16x32_bf16 v[44:47], v[124:127], v[116:119], v[44:47]\n"
    "ds_read_b128 v[156:159], v184 offset:27648\n"
    "v_mfma_f32_16x16x32_bf16 v[48:51], v[128:131], v[96:99], v[48:51]\n"
    "v_mfma_f32_16x16x32_bf16 v[52:55], v[128:131], v[100:103], v[52:55]\n"
    "ds_read_b64_tr_b16 v[172:173], v186 offset:20992\n"
    "v_mfma_f32_16x16x32_bf16 v[56:59], v[128:131], v[104:107], v[56:59]\n"
    "ds_read_b64_tr_b16 v[174:175], v186 offset:31232\n"
    "v_mfma_f32_16x16x32_bf16 v[60:63], v[128:131], v[108:111], v[60:63]\n"
    "v_mfma_f32_16x16x32_bf16 v[64:67], v[128:131], v[112:115], v[64:67]\n"
    "v_mfma_f32_16x16x32_bf16 v[68:71], v[128:131], v[116:119], v[68:71]\n"
    "v_mfma_f32_16x16x32_bf16 v[72:75], v[132:135], v[96:99], v[72:75]\n"
    "v_mfma_f32_16x16x32_bf16 v[76:79], v[132:135], v[100:103], v[76:79]\n"
    "v_mfma_f32_16x16x32_bf16 v[80:83], v[132:135], v[104:107], v[80:83]\n"
    "s_waitcnt lgkmcnt(0)\n"
    "v_mfma_f32_16x16x32_bf16 v[84:87], v[132:135], v[108:111], v[84:87]\n"
    "v_mfma_f32_16x16x32_bf16 v[88:91], v[132:135], v[112:115], v[88:91]\n"
    "v_mfma_f32_16x16x32_bf16 v[92:95], v[132:135], v[116:119], v[92:95]\n"
    "v_mfma_f32_16x16x32_bf16 v[0:3], v[160:163], v[136:139], v[0:3]\n"
    "v_mfma_f32_16x16x32_bf16 v[4:7], v[160:163], v[140:143], v[4:7]\n"
    "v_mfma_f32_16x16x32_bf16 v[8:11], v[160:163], v[144:147], v[8:11]\n"
    "v_mfma_f32_16x16x32_bf16 v[12:15], v[160:163], v[148:151], v[12:15]\n"
    "v_mfma_f32_16x16x32_bf16 v[16:19], v[160:163], v[152:155], v[16:19]\n"
    "v_mfma_f32_16x16x32_bf16 v[20:23], v[160:163], v[156:159], v[20:23]\n"
    "v_mfma_f32_16x16x32_bf16 v[24:27], v[164:167], v[136:139], v[24:27]\n"
    "v_mfma_f32_16x16x32_bf16 v[28:31], v[164:167], v[140:143], v[28:31]\n"
    "v_mfma_f32_16x16x32_bf16 v[32:35], v[164:167], v[144:147], v[32:35]\n"
    "v_mfma_f32_16x16x32_bf16 v[36:39], v[164:167], v[148:151], v[36:39]\n"
    "v_mfma_f32_16x16x32_bf16 v[40:43], v[164:167], v[152:155], v[40:43]\n"
    "v_mfma_f32_16x16x32_bf16 v[44:47], v[164:167], v[156:159], v[44:47]\n"
    "v_mfma_f32_16x16x32_bf16 v[48:51], v[168:171], v[136:139], v[48:51]\n"
    "v_mfma_f32_16x16x32_bf16 v[52:55], v[168:171], v[140:143], v[52:55]\n"
    "v_mfma_f32_16x16x32_bf16 v[56:59], v[168:171], v[144:147], v[56:59]\n"
    "v_mfma_f32_16x16x32_bf16 v[60:63], v[168:171], v[148:151], v[60:63]\n"
    "v_mfma_f32_16x16x32_bf16 v[64:67], v[168:171], v[152:155], v[64:67]\n"
    "v_mfma_f32_16x16x32_bf16 v[68:71], v[168:171], v[156:159], v[68:71]\n"
    "v_mfma_f32_16x16x32_bf16 v[72:75], v[172:175], v[136:139], v[72:75]\n"
    "v_mfma_f32_16x16x32_bf16 v[76:79], v[172:175], v[140:143], v[76:79]\n"
    "v_mfma_f32_16x16x32_bf16 v[80:83], v[172:175], v[144:147], v[80:83]\n"
    "v_mfma_f32_16x16x32_bf16 v[84:87], v[172:175], v[148:151], v[84:87]\n"
    "v_mfma_f32_16x16x32_bf16 v[88:91], v[172:175], v[152:155], v[88:91]\n"
    "v_mfma_f32_16x16x32_bf16 v[92:95], v[172:175], v[156:159], v[92:95]\n"
    "s_nop 15\n"
        : "+{v[0:31]}"(acc[0]), "+{v[32:63]}"(acc[1]), "+{v[64:95]}"(acc[2])
        : "{v[176:191]}"(od), [srdW] "s"(srdW), [srdO] "s"(srdO), [m0o] "s"(ldsb + 5120u * w), [wbeg] "s"(kq * (1024u * 1280u)), [obeg] "s"(kq * 32768u),
          [hl] "s"(__builtin_amdgcn_readfirstlane(hl)), [hn] "s"(hn)
        : "v96", "v97", "v98", "v99", "v100", "v101", "v102", "v103", "v104", "v105", "v106", "v107", "v108", "v109", "v110", "v111", "v112", "v113", "v114", "v115", "v116", "v117", "v118", "v119", "v120", "v121", "v122", "v123", "v124", "v125", "v126", "v127", "v128", "v129", "v130", "v131", "v132", "v133", "v134", "v135", "v136", "v137", "v138", "v139", "v140", "v141", "v142", "v143", "v144", "v145", "v146", "v147", "v148", "v149", "v150", "v151", "v152", "v153", "v154", "v155", "v156", "v157", "v158", "v159", "v160", "v161", "v162", "v163", "v164", "v165", "v166", "v167", "v168", "v169", "v170", "v171", "v172", "v173", "v174", "v175", "s64", "s65", "s66", "s67", "s68", "s69", "scc", "memory");
    lds_bar();
  }
  STAMP(9);
  if (kq == 0) exchange<0>(P, acc, w, br, i, mt, nt, tag);
  else if (kq == 1) exchange<1>(P, acc, w, br, i, mt, nt, tag);
  else if (kq == 2) exchange<2>(P, acc, w, br, i, mt, nt, tag);
  else exchange<3>(P, acc, w, br, i, mt, nt, tag);
}

__global__ __launch_bounds__(256, 2) void attn_kernel(Params P) {
  extern __shared__ __attribute__((aligned(16))) char lds[];
  const unsigned ldsb = (unsigned)(uintptr_t)(LAS char*)lds;
  const unsigned w = __builtin_amdgcn_readfirstlane(threadIdx.x >> 6);
  const unsigned p = blockIdx.x, role = p >> 8, g = p & 7u, i5 = (p >> 3) & 31u;
  const unsigned br = g >> 2, b = i5 >> 2, j = i5 & 3u, h = 8u * (g & 3u) + 4u * role + j;
  const unsigned ridx = (b + 2u * j) & 7u;
  const unsigned ch0 = 2u * ((0x98654310u >> (4u * ridx)) & 15u);
  set_prio<PRIOQ0, PRIOQ1>(role);
  unsigned long long tag;
  const unsigned lane = threadIdx.x & 63u;
  const unsigned l31 = lane & 31u, hh = lane >> 5, l15 = lane & 15u, g4 = lane >> 4;
  const unsigned prow = (lane >> 2) & 7u, ppos = lane & 3u;
  const unsigned tq = (lane >> 2) & 3u, dsub = (lane >> 4) & 1u;

  KvDma s;
  const rsrc_t srdP = __builtin_amdgcn_make_buffer_rsrc((void*)((br ? P.wo[1] : P.wo[0]) + (size_t)((g & 3u) * 1024u + (p >> 3) * 16u) * 1280u), 0, 20480, 0x00020000);
  u32x32 qf = lane;

  {
    const rsrc_t srdX = __builtin_amdgcn_make_buffer_rsrc((void*)((br ? P.x[1] : P.x[0]) + (size_t)b * (SQ * DM * 2) - 4096), 0, (SQ * DM * 2 + 4096), 0x00020000);
    const rsrc_t srdW = __builtin_amdgcn_make_buffer_rsrc((void*)((br ? P.wq[1] : P.wq[0]) - 4096), 0, (DM * 4096 * 2 + 4096), 0x00020000);
    u32x8 qs = lane, qd = lane, qw;
    qs[0] = (32u * w + (lane >> 2)) * 1280u + 16u * (ppos ^ ((0u - g4) & 3u)) + (4096u - 2304u); qs[1] = qs[0] + 20480u - 1024u;
    qs[2] = (128u + 4u * w + (lane >> 2)) * 1280u + 16u * (ppos ^ ((0u - w) & 3u)) + 4096u;
    qs[3] = (8u * w + prow) * 8192u + 256u * h + 64u * hh + 16u * (ppos ^ (2u * (w & 1u))) + (4096u - 256u); qs[4] = qs[3] + 128u - 1024u;
    unsigned sx;
    asm volatile(
    "s_lshl_b32 s64, %[ch0], 6\n"
    "s_lshl_b32 s65, %[ch0], 18\n"
    "s_add_u32 m0, %[m0q], 0\n"
    "s_nop 0\n"
    "buffer_load_dwordx4 v171, %[srdW], s65 offen offset:256 lds\n"
    "buffer_load_dwordx4 v172, %[srdW], s65 offen offset:1280 lds\n"
    "buffer_load_dwordx4 v168, %[srdX], s64 offen offset:2304 lds\n"
    "buffer_load_dwordx4 v169, %[srdX], s64 offen offset:3328 lds\n"
    "s_mov_b64 exec, 0xffff\n"
    "buffer_load_dwordx4 v170, %[srdX], s64 offen offset:0 lds\n"
    "s_mov_b64 exec, -1\n"
    "s_add_u32 s64, s64, 64\n"
    "s_cmp_eq_u32 s64, 1280\n"
    "s_cselect_b32 s64, 0, s64\n"
    "s_lshl_b32 s65, s64, 12\n"
    "s_add_u32 m0, %[m0q], 17408\n"
    "s_nop 0\n"
    "buffer_load_dwordx4 v171, %[srdW], s65 offen offset:256 lds\n"
    "buffer_load_dwordx4 v172, %[srdW], s65 offen offset:1280 lds\n"
    "buffer_load_dwordx4 v168, %[srdX], s64 offen offset:2304 lds\n"
    "buffer_load_dwordx4 v169, %[srdX], s64 offen offset:3328 lds\n"
    "s_mov_b64 exec, 0xffff\n"
    "buffer_load_dwordx4 v170, %[srdX], s64 offen offset:0 lds\n"
    "s_mov_b64 exec, -1\n"
    "s_add_u32 s64, s64, 64\n"
    "s_cmp_eq_u32 s64, 1280\n"
    "s_cselect_b32 s64, 0, s64\n"
    "s_lshl_b32 s65, s64, 12\n"
    "s_add_u32 m0, %[m0q], 34816\n"
    "s_nop 0\n"
    "buffer_load_dwordx4 v171, %[srdW], s65 offen offset:256 lds\n"
    "buffer_load_dwordx4 v172, %[srdW], s65 offen offset:1280 lds\n"
    "buffer_load_dwordx4 v168, %[srdX], s64 offen offset:2304 lds\n"
    "buffer_load_dwordx4 v169, %[srdX], s64 offen offset:3328 lds\n"
    "s_mov_b64 exec, 0xffff\n"
    "buffer_load_dwordx4 v170, %[srdX], s64 offen offset:0 lds\n"
    "s_mov_b64 exec, -1\n"
    "s_add_u32 s64, s64, 64\n"
    "s_cmp_eq_u32 s64, 1280\n"
    "s_cselect_b32 s64, 0, s64\n"
    "s_lshl_b32 s65, s64, 12\n"
    "s_add_u32 m0, %[m0q], 52224\n"
    "s_nop 0\n"
    "buffer_load_dwordx4 v171, %[srdW], s65 offen offset:256 lds\n"
    "buffer_load_dwordx4 v172, %[srdW], s65 offen offset:1280 lds\n"
    "buffer_load_dwordx4 v168, %[srdX], s64 offen offset:2304 lds\n"
    "buffer_load_dwordx4 v169, %[srdX], s64 offen offset:3328 lds\n"
    "s_mov_b64 exec, 0xffff\n"
    "buffer_load_dwordx4 v170, %[srdX], s64 offen offset:0 lds\n"
    "s_mov_b64 exec, -1\n"
    "s_add_u32 s64, s64, 64\n"
    "s_cmp_eq_u32 s64, 1280\n"
    "s_cselect_b32 s64, 0, s64\n"
    "s_lshl_b32 s65, s64, 12\n"
    "s_mov_b32 %[sx], s64\n"
        : [sx] "=s"(sx) : "{v[168:175]}"(qs), [srdX] "s"(srdX), [srdW] "s"(srdW), [m0q] "s"(ldsb + 4352u * w), [ch0] "s"(ch0) : "s64", "s65", "scc", "memory");
    SB();
    s.srdK = __builtin_amdgcn_make_buffer_rsrc((void*)((br ? P.k[1] : P.k[0]) + (size_t)(b * NH + h) * (SK * DH * 2)), 0, KVREC, 0x00020000);
    s.srdV = __builtin_amdgcn_make_buffer_rsrc((void*)((br ? P.v[1] : P.v[0]) + (size_t)(b * NH + h) * (SK * DH * 2)), 0, KVREC, 0x00020000);
    s.kv0 = prow * 256u + 64u * hh + 16u * (ppos ^ (3u * (prow >> 2))); s.kv1 = s.kv0 ^ 32u;
    s.vv = prow * 256u + 64u * hh + 16u * (ppos ^ (2u * (prow >> 2)));
    s.m0w = ldsb + 4096u * w; s.sow = 4096u * w;
    qd[0] = ldsb + 4352u * w + 2304u + 64u * l15 + 16u * (g4 ^ ((0u - (l15 >> 2)) & 3u));
    qd[1] = ldsb + 4352u * (l15 >> 2) + 64u * (l15 & 3u) + 16u * (g4 ^ ((0u - (l15 >> 2)) & 3u));
    qd[2] = s.kv0; qd[3] = s.kv1; qd[4] = s.vv;
#pragma unroll
    for (int i = 0; i < 4; ++i) {
      qw[2 * i] = ldsb + 256u + 4352u * g4 + 512u * (w ^ i) + 32u * (g4 & 1u) + 64u * tq + 8u * ppos; qw[2 * i + 1] = qw[2 * i] ^ 32u;
    }
    f32x32 q01, q23; f32x8 qx;
    asm volatile(
    "s_load_dwordx2 %[epoch], %[sync], 0x0\n"
    "s_mov_b32 s64, %[sx]\n"
    "s_lshl_b32 s65, %[sx], 12\n"
    "s_add_u32 s67, %[sow], 0x4000\n"
    "v_mov_b32 v64, 0\n"
    "v_mov_b32 v65, 0\n"
    "v_mov_b32 v66, 0\n"
    "v_mov_b32 v67, 0\n"
    "v_mov_b32 v68, 0\n"
    "v_mov_b32 v69, 0\n"
    "v_mov_b32 v70, 0\n"
    "v_mov_b32 v71, 0\n"
    "s_nop 1\n"
    "v_mfma_f32_32x32x16_bf16 v[0:15], v[64:67], v[64:67], 0\n"
    "v_mfma_f32_32x32x16_bf16 v[16:31], v[64:67], v[64:67], 0\n"
    "v_mfma_f32_32x32x16_bf16 v[32:47], v[64:67], v[64:67], 0\n"
    "v_mfma_f32_32x32x16_bf16 v[48:63], v[64:67], v[64:67], 0\n"
    "v_mfma_f32_32x32x16_bf16 v[116:131], v[64:67], v[64:67], 0\n"
    "v_mfma_f32_32x32x16_bf16 v[144:159], v[64:67], v[64:67], 0\n"
    "s_waitcnt vmcnt(15) lgkmcnt(0)\n"
    "s_barrier\n"
    "ds_read_b64_tr_b16 v[84:85], v160 offset:0\n"
    "ds_read_b64_tr_b16 v[86:87], v160 offset:256\n"
    "ds_read_b128 v[72:75], v176 offset:0\n"
    "ds_read_b128 v[80:83], v177 offset:0\n"
    "ds_read_b64_tr_b16 v[88:89], v161 offset:0\n"
    "ds_read_b64_tr_b16 v[90:91], v161 offset:256\n"
    "ds_read_b64_tr_b16 v[92:93], v162 offset:0\n"
    "ds_read_b64_tr_b16 v[94:95], v162 offset:256\n"
    "ds_read_b64_tr_b16 v[96:97], v163 offset:0\n"
    "ds_read_b64_tr_b16 v[98:99], v163 offset:256\n"
    "ds_read_b64_tr_b16 v[100:101], v164 offset:0\n"
    "ds_read_b64_tr_b16 v[102:103], v164 offset:256\n"
    "ds_read_b128 v[76:79], v176 offset:1024\n"
    "ds_read_b64_tr_b16 v[104:105], v165 offset:0\n"
    "ds_read_b64_tr_b16 v[106:107], v165 offset:256\n"
    "ds_read_b64_tr_b16 v[108:109], v166 offset:0\n"
    "ds_read_b64_tr_b16 v[110:111], v166 offset:256\n"
    "ds_read_b64_tr_b16 v[112:113], v167 offset:0\n"
    "ds_read_b64_tr_b16 v[114:115], v167 offset:256\n"
    "s_mov_b32 s66, 4\n"
    ".Lq_%=:\n"
    "s_waitcnt vmcnt(10) lgkmcnt(0)\n"
    "s_barrier\n"
    "s_add_u32 m0, %[m0q], 0\n"
    "v_mfma_f32_16x16x32_bf16 v[56:59], v[152:155], v[120:123], v[56:59]\n"
    "ds_read_b64_tr_b16 v[128:129], v160 offset:17408\n"
    "ds_read_b64_tr_b16 v[130:131], v160 offset:17664\n"
    "v_mfma_f32_16x16x32_bf16 v[28:31], v[156:159], v[116:119], v[28:31]\n"
    "ds_read_b128 v[116:119], v176 offset:17408\n"
    "buffer_load_dwordx4 v171, %[srdW], s65 offen offset:256 lds\n"
    "v_mfma_f32_16x16x32_bf16 v[60:63], v[156:159], v[120:123], v[60:63]\n"
    "ds_read_b128 v[124:127], v177 offset:17408\n"
    "ds_read_b64_tr_b16 v[132:133], v161 offset:17408\n"
    "v_mfma_f32_16x16x32_bf16 v[0:3], v[84:87], v[72:75], v[0:3]\n"
    "ds_read_b64_tr_b16 v[134:135], v161 offset:17664\n"
    "ds_read_b64_tr_b16 v[136:137], v162 offset:17408\n"
    "v_mfma_f32_16x16x32_bf16 v[32:35], v[84:87], v[76:79], v[32:35]\n"
    "ds_read_b64_tr_b16 v[138:139], v162 offset:17664\n"
    "buffer_load_dwordx4 v172, %[srdW], s65 offen offset:1280 lds\n"
    "v_mfma_f32_16x16x32_bf16 v[64:67], v[84:87], v[80:83], v[64:67]\n"
    "ds_read_b64_tr_b16 v[140:141], v163 offset:17408\n"
    "ds_read_b64_tr_b16 v[142:143], v163 offset:17664\n"
    "v_mfma_f32_16x16x32_bf16 v[4:7], v[88:91], v[72:75], v[4:7]\n"
    "ds_read_b64_tr_b16 v[144:145], v164 offset:17408\n"
    "v_mfma_f32_16x16x32_bf16 v[36:39], v[88:91], v[76:79], v[36:39]\n"
    "ds_read_b64_tr_b16 v[146:147], v164 offset:17664\n"
    "v_mfma_f32_16x16x32_bf16 v[68:71], v[88:91], v[80:83], v[68:71]\n"
    "ds_read_b128 v[120:123], v176 offset:18432\n"
    "ds_read_b64_tr_b16 v[148:149], v165 offset:17408\n"
    "buffer_load_dwordx4 v168, %[srdX], s64 offen offset:2304 lds\n"
    "v_mfma_f32_16x16x32_bf16 v[8:11], v[92:95], v[72:75], v[8:11]\n"
    "ds_read_b64_tr_b16 v[150:151], v165 offset:17664\n"
    "v_mfma_f32_16x16x32_bf16 v[40:43], v[92:95], v[76:79], v[40:43]\n"
    "ds_read_b64_tr_b16 v[152:153], v166 offset:17408\n"
    "ds_read_b64_tr_b16 v[154:155], v166 offset:17664\n"
    "v_mfma_f32_16x16x32_bf16 v[12:15], v[96:99], v[72:75], v[12:15]\n"
    "buffer_load_dwordx4 v169, %[srdX], s64 offen offset:3328 lds\n"
    "v_mfma_f32_16x16x32_bf16 v[44:47], v[96:99], v[76:79], v[44:47]\n"
    "ds_read_b64_tr_b16 v[156:157], v167 offset:17408\n"
    "ds_read_b64_tr_b16 v[158:159], v167 offset:17664\n"
    "v_mfma_f32_16x16x32_bf16 v[16:19], v[100:103], v[72:75], v[16:19]\n"
    "v_mfma_f32_16x16x32_bf16 v[48:51], v[100:103], v[76:79], v[48:51]\n"
    "s_mov_b64 exec, 0xffff\n"
    "buffer_load_dwordx4 v170, %[srdX], s64 offen offset:0 lds\n"
    "s_mov_b64 exec, -1\n"
    "v_mfma_f32_16x16x32_bf16 v[20:23], v[104:107], v[72:75], v[20:23]\n"
    "s_add_u32 s64, s64, 64\n"
    "s_cmp_eq_u32 s64, 1280\n"
    "s_cselect_b32 s64, 0, s64\n"
    "s_lshl_b32 s65, s64, 12\n"
    "v_mfma_f32_16x16x32_bf16 v[52:55], v[104:107], v[76:79], v[52:55]\n"
    "v_mfma_f32_16x16x32_bf16 v[24:27], v[108:111], v[72:75], v[24:27]\n"
    "s_waitcnt vmcnt(10) lgkmcnt(0)\n"
    "s_barrier\n"
    "s_add_u32 m0, %[m0q], 17408\n"
    "v_mfma_f32_16x16x32_bf16 v[56:59], v[108:111], v[76:79], v[56:59]\n"
    "ds_read_b64_tr_b16 v[84:85], v160 offset:34816\n"
    "ds_read_b64_tr_b16 v[86:87], v160 offset:35072\n"
    "v_mfma_f32_16x16x32_bf16 v[28:31], v[112:115], v[72:75], v[28:31]\n"
    "ds_read_b128 v[72:75], v176 offset:34816\n"
    "buffer_load_dwordx4 v171, %[srdW], s65 offen offset:256 lds\n"
    "v_mfma_f32_16x16x32_bf16 v[60:63], v[112:115], v[76:79], v[60:63]\n"
    "ds_read_b128 v[80:83], v177 offset:34816\n"
    "ds_read_b64_tr_b16 v[88:89], v161 offset:34816\n"
    "v_mfma_f32_16x16x32_bf16 v[0:3], v[128:131], v[116:119], v[0:3]\n"
    "ds_read_b64_tr_b16 v[90:91], v161 offset:35072\n"
    "ds_read_b64_tr_b16 v[92:93], v162 offset:34816\n"
    "v_mfma_f32_16x16x32_bf16 v[32:35], v[128:131], v[120:123], v[32:35]\n"
    "ds_read_b64_tr_b16 v[94:95], v162 offset:35072\n"
    "buffer_load_dwordx4 v172, %[srdW], s65 offen offset:1280 lds\n"
    "v_mfma_f32_16x16x32_bf16 v[64:67], v[128:131], v[124:127], v[64:67]\n"
    "ds_read_b64_tr_b16 v[96:97], v163 offset:34816\n"
    "ds_read_b64_tr_b16 v[98:99], v163 offset:35072\n"
    "v_mfma_f32_16x16x32_bf16 v[4:7], v[132:135], v[116:119], v[4:7]\n"
    "ds_read_b64_tr_b16 v[100:101], v164 offset:34816\n"
    "v_mfma_f32_16x16x32_bf16 v[36:39], v[132:135], v[120:123], v[36:39]\n"
    "ds_read_b64_tr_b16 v[102:103], v164 offset:35072\n"
    "v_mfma_f32_16x16x32_bf16 v[68:71], v[132:135], v[124:127], v[68:71]\n"
    "ds_read_b128 v[76:79], v176 offset:35840\n"
    "ds_read_b64_tr_b16 v[104:105], v165 offset:34816\n"
    "buffer_load_dwordx4 v168, %[srdX], s64 offen offset:2304 lds\n"
    "v_mfma_f32_16x16x32_bf16 v[8:11], v[136:139], v[116:119], v[8:11]\n"
    "ds_read_b64_tr_b16 v[106:107], v165 offset:35072\n"
    "v_mfma_f32_16x16x32_bf16 v[40:43], v[136:139], v[120:123], v[40:43]\n"
    "ds_read_b64_tr_b16 v[108:109], v166 offset:34816\n"
    "ds_read_b64_tr_b16 v[110:111], v166 offset:35072\n"
    "v_mfma_f32_16x16x32_bf16 v[12:15], v[140:143], v[116:119], v[12:15]\n"
    "buffer_load_dwordx4 v169, %[srdX], s64 offen offset:3328 lds\n"
    "v_mfma_f32_16x16x32_bf16 v[44:47], v[140:143], v[120:123], v[44:47]\n"
    "ds_read_b64_tr_b16 v[112:113], v167 offset:34816\n"
    "ds_read_b64_tr_b16 v[114:115], v167 offset:35072\n"
    "v_mfma_f32_16x16x32_bf16 v[16:19], v[144:147], v[116:119], v[16:19]\n"
    "v_mfma_f32_16x16x32_bf16 v[48:51], v[144:147], v[120:123], v[48:51]\n"
    "s_mov_b64 exec, 0xffff\n"
    "buffer_load_dwordx4 v170, %[srdX], s64 offen offset:0 lds\n"
    "s_mov_b64 exec, -1\n"
    "v_mfma_f32_16x16x32_bf16 v[20:23], v[148:151], v[116:119], v[20:23]\n"
    "s_add_u32 s64, s64, 64\n"
    "s_cmp_eq_u32 s64, 1280\n"
    "s_cselect_b32 s64, 0, s64\n"
    "s_lshl_b32 s65, s64, 12\n"
    "v_mfma_f32_16x16x32_bf16 v[52:55], v[148:151], v[120:123], v[52:55]\n"
    "v_mfma_f32_16x16x32_bf16 v[24:27], v[152:155], v[116:119], v[24:27]\n"
    "s_waitcnt vmcnt(10) lgkmcnt(0)\n"
    "s_barrier\n"
    "s_add_u32 m0, %[m0q], 34816\n"
    "v_mfma_f32_16x16x32_bf16 v[56:59], v[152:155], v[120:123], v[56:59]\n"
    "ds_read_b64_tr_b16 v[128:129], v160 offset:52224\n"
    "ds_read_b64_tr_b16 v[130:131], v160 offset:52480\n"
    "v_mfma_f32_16x16x32_bf16 v[28:31], v[156:159], v[116:119], v[28:31]\n"
    "ds_read_b128 v[116:119], v176 offset:52224\n"
    "buffer_load_dwordx4 v171, %[srdW], s65 offen offset:256 lds\n"
    "v_mfma_f32_16x16x32_bf16 v[60:63], v[156:159], v[120:123], v[60:63]\n"
    "ds_read_b128 v[124:127], v177 offset:52224\n"
    "ds_read_b64_tr_b16 v[132:133], v161 offset:52224\n"
    "v_mfma_f32_16x16x32_bf16 v[0:3], v[84:87], v[72:75], v[0:3]\n"
    "ds_read_b64_tr_b16 v[134:135], v161 offset:52480\n"
    "ds_read_b64_tr_b16 v[136:137], v162 offset:52224\n"
    "v_mfma_f32_16x16x32_bf16 v[32:35], v[84:87], v[76:79], v[32:35]\n"
    "ds_read_b64_tr_b16 v[138:139], v162 offset:52480\n"
    "buffer_load_dwordx4 v172, %[srdW], s65 offen offset:1280 lds\n"
    "v_mfma_f32_16x16x32_bf16 v[64:67], v[84:87], v[80:83], v[64:67]\n"
    "ds_read_b64_tr_b16 v[140:141], v163 offset:52224\n"
    "ds_read_b64_tr_b16 v[142:143], v163 offset:52480\n"
    "v_mfma_f32_16x16x32_bf16 v[4:7], v[88:91], v[72:75], v[4:7]\n"
    "ds_read_b64_tr_b16 v[144:145], v164 offset:52224\n"
    "v_mfma_f32_16x16x32_bf16 v[36:39], v[88:91], v[76:79], v[36:39]\n"
    "ds_read_b64_tr_b16 v[146:147], v164 offset:52480\n"
    "v_mfma_f32_16x16x32_bf16 v[68:71], v[88:91], v[80:83], v[68:71]\n"
    "ds_read_b128 v[120:123], v176 offset:53248\n"
    "ds_read_b64_tr_b16 v[148:149], v165 offset:52224\n"
    "buffer_load_dwordx4 v168, %[srdX], s64 offen offset:2304 lds\n"
    "v_mfma_f32_16x16x32_bf16 v[8:11], v[92:95], v[72:75], v[8:11]\n"
    "ds_read_b64_tr_b16 v[150:151], v165 offset:52480\n"
    "v_mfma_f32_16x16x32_bf16 v[40:43], v[92:95], v[76:79], v[40:43]\n"
    "ds_read_b64_tr_b16 v[152:153], v166 offset:52224\n"
    "ds_read_b64_tr_b16 v[154:155], v166 offset:52480\n"
    "v_mfma_f32_16x16x32_bf16 v[12:15], v[96:99], v[72:75], v[12:15]\n"
    "buffer_load_dwordx4 v169, %[srdX], s64 offen offset:3328 lds\n"
    "v_mfma_f32_16x16x32_bf16 v[44:47], v[96:99], v[76:79], v[44:47]\n"
    "ds_read_b64_tr_b16 v[156:157], v167 offset:52224\n"
    "ds_read_b64_tr_b16 v[158:159], v167 offset:52480\n"
    "v_mfma_f32_16x16x32_bf16 v[16:19], v[100:103], v[72:75], v[16:19]\n"
    "v_mfma_f32_16x16x32_bf16 v[48:51], v[100:103], v[76:79], v[48:51]\n"
    "s_mov_b64 exec, 0xffff\n"
    "buffer_load_dwordx4 v170, %[srdX], s64 offen offset:0 lds\n"
    "s_mov_b64 exec, -1\n"
    "v_mfma_f32_16x16x32_bf16 v[20:23], v[104:107], v[72:75], v[20:23]\n"
    "s_add_u32 s64, s64, 64\n"
    "s_cmp_eq_u32 s64, 1280\n"
    "s_cselect_b32 s64, 0, s64\n"
    "s_lshl_b32 s65, s64, 12\n"
    "v_mfma_f32_16x16x32_bf16 v[52:55], v[104:107], v[76:79], v[52:55]\n"
    "v_mfma_f32_16x16x32_bf16 v[24:27], v[108:111], v[72:75], v[24:27]\n"
    "s_waitcnt vmcnt(10) lgkmcnt(0)\n"
    "s_barrier\n"
    "s_add_u32 m0, %[m0q], 52224\n"
    "v_mfma_f32_16x16x32_bf16 v[56:59], v[108:111], v[76:79], v[56:59]\n"
    "ds_read_b64_tr_b16 v[84:85], v160 offset:0\n"
    "ds_read_b64_tr_b16 v[86:87], v160 offset:256\n"
    "v_mfma_f32_16x16x32_bf16 v[28:31], v[112:115], v[72:75], v[28:31]\n"
    "ds_read_b128 v[72:75], v176 offset:0\n"
    "buffer_load_dwordx4 v171, %[srdW], s65 offen offset:256 lds\n"
    "v_mfma_f32_16x16x32_bf16 v[60:63], v[112:115], v[76:79], v[60:63]\n"
    "ds_read_b128 v[80:83], v177 offset:0\n"
    "ds_read_b64_tr_b16 v[88:89], v161 offset:0\n"
    "v_mfma_f32_16x16x32_bf16 v[0:3], v[128:131], v[116:119], v[0:3]\n"
    "ds_read_b64_tr_b16 v[90:91], v161 offset:256\n"
    "ds_read_b64_tr_b16 v[92:93], v162 offset:0\n"
    "v_mfma_f32_16x16x32_bf16 v[32:35], v[128:131], v[120:123], v[32:35]\n"
    "ds_read_b64_tr_b16 v[94:95], v162 offset:256\n"
    "buffer_load_dwordx4 v172, %[srdW], s65 offen offset:1280 lds\n"
    "v_mfma_f32_16x16x32_bf16 v[64:67], v[128:131], v[124:127], v[64:67]\n"
    "ds_read_b64_tr_b16 v[96:97], v163 offset:0\n"
    "ds_read_b64_tr_b16 v[98:99], v163 offset:256\n"
    "v_mfma_f32_16x16x32_bf16 v[4:7], v[132:135], v[116:119], v[4:7]\n"
    "ds_read_b64_tr_b16 v[100:101], v164 offset:0\n"
    "v_mfma_f32_16x16x32_bf16 v[36:39], v[132:135], v[120:123], v[36:39]\n"
    "ds_read_b64_tr_b16 v[102:103], v164 offset:256\n"
    "v_mfma_f32_16x16x32_bf16 v[68:71], v[132:135], v[124:127], v[68:71]\n"
    "ds_read_b128 v[76:79], v176 offset:1024\n"
    "ds_read_b64_tr_b16 v[104:105], v165 offset:0\n"
    "buffer_load_dwordx4 v168, %[srdX], s64 offen offset:2304 lds\n"
    "v_mfma_f32_16x16x32_bf16 v[8:11], v[136:139], v[116:119], v[8:11]\n"
    "ds_read_b64_tr_b16 v[106:107], v165 offset:256\n"
    "v_mfma_f32_16x16x32_bf16 v[40:43], v[136:139], v[120:123], v[40:43]\n"
    "ds_read_b64_tr_b16 v[108:109], v166 offset:0\n"
    "ds_read_b64_tr_b16 v[110:111], v166 offset:256\n"
    "v_mfma_f32_16x16x32_bf16 v[12:15], v[140:143], v[116:119], v[12:15]\n"
    "buffer_load_dwordx4 v169, %[srdX], s64 offen offset:3328 lds\n"
    "v_mfma_f32_16x16x32_bf16 v[44:47], v[140:143], v[120:123], v[44:47]\n"
    "ds_read_b64_tr_b16 v[112:113], v167 offset:0\n"
    "ds_read_b64_tr_b16 v[114:115], v167 offset:256\n"
    "v_mfma_f32_16x16x32_bf16 v[16:19], v[144:147], v[116:119], v[16:19]\n"
    "v_mfma_f32_16x16x32_bf16 v[48:51], v[144:147], v[120:123], v[48:51]\n"
    "s_mov_b64 exec, 0xffff\n"
    "buffer_load_dwordx4 v170, %[srdX], s64 offen offset:0 lds\n"
    "s_mov_b64 exec, -1\n"
    "v_mfma_f32_16x16x32_bf16 v[20:23], v[148:151], v[116:119], v[20:23]\n"
    "s_add_u32 s64, s64, 64\n"
    "s_cmp_eq_u32 s64, 1280\n"
    "s_cselect_b32 s64, 0, s64\n"
    "s_lshl_b32 s65, s64, 12\n"
    "v_mfma_f32_16x16x32_bf16 v[52:55], v[148:151], v[120:123], v[52:55]\n"
    "v_mfma_f32_16x16x32_bf16 v[24:27], v[152:155], v[116:119], v[24:27]\n"
    "s_sub_u32 s66, s66, 1\n"
    "s_cmp_lg_u32 s66, 0\n"
    "s_cbranch_scc1 .Lq_%=\n"
    "s_waitcnt vmcnt(10) lgkmcnt(0)\n"
    "s_barrier\n"
    "s_add_u32 m0, %[m0w], 0\n"
    "v_mfma_f32_16x16x32_bf16 v[56:59], v[152:155], v[120:123], v[56:59]\n"
    "ds_read_b64_tr_b16 v[128:129], v160 offset:17408\n"
    "ds_read_b64_tr_b16 v[130:131], v160 offset:17664\n"
    "v_mfma_f32_16x16x32_bf16 v[28:31], v[156:159], v[116:119], v[28:31]\n"
    "ds_read_b128 v[116:119], v176 offset:17408\n"
    "buffer_load_dwordx4 v178, %[srdK], %[sow] offen offset:0 nt lds\n"
    "v_mfma_f32_16x16x32_bf16 v[60:63], v[156:159], v[120:123], v[60:63]\n"
    "ds_read_b128 v[124:127], v177 offset:17408\n"
    "ds_read_b64_tr_b16 v[132:133], v161 offset:17408\n"
    "v_mfma_f32_16x16x32_bf16 v[0:3], v[84:87], v[72:75], v[0:3]\n"
    "ds_read_b64_tr_b16 v[134:135], v161 offset:17664\n"
    "ds_read_b64_tr_b16 v[136:137], v162 offset:17408\n"
    "v_mfma_f32_16x16x32_bf16 v[32:35], v[84:87], v[76:79], v[32:35]\n"
    "ds_read_b64_tr_b16 v[138:139], v162 offset:17664\n"
    "buffer_load_dwordx4 v179, %[srdK], %[sow] offen offset:2048 nt lds\n"
    "v_mfma_f32_16x16x32_bf16 v[64:67], v[84:87], v[80:83], v[64:67]\n"
    "ds_read_b64_tr_b16 v[140:141], v163 offset:17408\n"
    "ds_read_b64_tr_b16 v[142:143], v163 offset:17664\n"
    "v_mfma_f32_16x16x32_bf16 v[4:7], v[88:91], v[72:75], v[4:7]\n"
    "ds_read_b64_tr_b16 v[144:145], v164 offset:17408\n"
    "v_mfma_f32_16x16x32_bf16 v[36:39], v[88:91], v[76:79], v[36:39]\n"
    "ds_read_b64_tr_b16 v[146:147], v164 offset:17664\n"
    "v_mfma_f32_16x16x32_bf16 v[68:71], v[88:91], v[80:83], v[68:71]\n"
    "ds_read_b128 v[120:123], v176 offset:18432\n"
    "ds_read_b64_tr_b16 v[148:149], v165 offset:17408\n"
    "s_add_u32 m0, %[m0w], 896\n"
    "s_nop 0\n"
    "buffer_load_dwordx4 v178, %[srdK], %[sow] offen offset:128 nt lds\n"
    "v_mfma_f32_16x16x32_bf16 v[8:11], v[92:95], v[72:75], v[8:11]\n"
    "ds_read_b64_tr_b16 v[150:151], v165 offset:17664\n"
    "v_mfma_f32_16x16x32_bf16 v[40:43], v[92:95], v[76:79], v[40:43]\n"
    "ds_read_b64_tr_b16 v[152:153], v166 offset:17408\n"
    "ds_read_b64_tr_b16 v[154:155], v166 offset:17664\n"
    "v_mfma_f32_16x16x32_bf16 v[12:15], v[96:99], v[72:75], v[12:15]\n"
    "buffer_load_dwordx4 v179, %[srdK], %[sow] offen offset:2176 nt lds\n"
    "v_mfma_f32_16x16x32_bf16 v[44:47], v[96:99], v[76:79], v[44:47]\n"
    "ds_read_b64_tr_b16 v[156:157], v167 offset:17408\n"
    "ds_read_b64_tr_b16 v[158:159], v167 offset:17664\n"
    "v_mfma_f32_16x16x32_bf16 v[16:19], v[100:103], v[72:75], v[16:19]\n"
    "v_mfma_f32_16x16x32_bf16 v[48:51], v[100:103], v[76:79], v[48:51]\n"
    "v_mfma_f32_16x16x32_bf16 v[20:23], v[104:107], v[72:75], v[20:23]\n"
    "v_mfma_f32_16x16x32_bf16 v[52:55], v[104:107], v[76:79], v[52:55]\n"
    "v_mfma_f32_16x16x32_bf16 v[24:27], v[108:111], v[72:75], v[24:27]\n"
    "s_waitcnt vmcnt(9) lgkmcnt(0)\n"
    "s_barrier\n"
    "s_add_u32 m0, %[m0w], 16384\n"
    "v_mfma_f32_16x16x32_bf16 v[56:59], v[108:111], v[76:79], v[56:59]\n"
    "ds_read_b64_tr_b16 v[84:85], v160 offset:34816\n"
    "ds_read_b64_tr_b16 v[86:87], v160 offset:35072\n"
    "v_mfma_f32_16x16x32_bf16 v[28:31], v[112:115], v[72:75], v[28:31]\n"
    "ds_read_b128 v[72:75], v176 offset:34816\n"
    "buffer_load_dwordx4 v180, %[srdV], %[sow] offen offset:0 nt lds\n"
    "v_mfma_f32_16x16x32_bf16 v[60:63], v[112:115], v[76:79], v[60:63]\n"
    "ds_read_b128 v[80:83], v177 offset:34816\n"
    "ds_read_b64_tr_b16 v[88:89], v161 offset:34816\n"
    "v_mfma_f32_16x16x32_bf16 v[0:3], v[128:131], v[116:119], v[0:3]\n"
    "ds_read_b64_tr_b16 v[90:91], v161 offset:35072\n"
    "ds_read_b64_tr_b16 v[92:93], v162 offset:34816\n"
    "v_mfma_f32_16x16x32_bf16 v[32:35], v[128:131], v[120:123], v[32:35]\n"
    "ds_read_b64_tr_b16 v[94:95], v162 offset:35072\n"
    "buffer_load_dwordx4 v180, %[srdV], %[sow] offen offset:2048 nt lds\n"
    "v_mfma_f32_16x16x32_bf16 v[64:67], v[128:131], v[124:127], v[64:67]\n"
    "ds_read_b64_tr_b16 v[96:97], v163 offset:34816\n"
    "ds_read_b64_tr_b16 v[98:99], v163 offset:35072\n"
    "v_mfma_f32_16x16x32_bf16 v[4:7], v[132:135], v[116:119], v[4:7]\n"
    "ds_read_b64_tr_b16 v[100:101], v164 offset:34816\n"
    "v_mfma_f32_16x16x32_bf16 v[36:39], v[132:135], v[120:123], v[36:39]\n"
    "ds_read_b64_tr_b16 v[102:103], v164 offset:35072\n"
    "v_mfma_f32_16x16x32_bf16 v[68:71], v[132:135], v[124:127], v[68:71]\n"
    "ds_read_b128 v[76:79], v176 offset:35840\n"
    "ds_read_b64_tr_b16 v[104:105], v165 offset:34816\n"
    "s_add_u32 m0, %[m0w], 17280\n"
    "s_nop 0\n"
    "buffer_load_dwordx4 v180, %[srdV], %[sow] offen offset:128 nt lds\n"
    "v_mfma_f32_16x16x32_bf16 v[8:11], v[136:139], v[116:119], v[8:11]\n"
    "ds_read_b64_tr_b16 v[106:107], v165 offset:35072\n"
    "v_mfma_f32_16x16x32_bf16 v[40:43], v[136:139], v[120:123], v[40:43]\n"
    "ds_read_b64_tr_b16 v[108:109], v166 offset:34816\n"
    "ds_read_b64_tr_b16 v[110:111], v166 offset:35072\n"
    "v_mfma_f32_16x16x32_bf16 v[12:15], v[140:143], v[116:119], v[12:15]\n"
    "buffer_load_dwordx4 v180, %[srdV], %[sow] offen offset:2176 nt lds\n"
    "v_mfma_f32_16x16x32_bf16 v[44:47], v[140:143], v[120:123], v[44:47]\n"
    "ds_read_b64_tr_b16 v[112:113], v167 offset:34816\n"
    "ds_read_b64_tr_b16 v[114:115], v167 offset:35072\n"
    "v_mfma_f32_16x16x32_bf16 v[16:19], v[144:147], v[116:119], v[16:19]\n"
    "v_mfma_f32_16x16x32_bf16 v[48:51], v[144:147], v[120:123], v[48:51]\n"
    "v_mfma_f32_16x16x32_bf16 v[20:23], v[148:151], v[116:119], v[20:23]\n"
    "v_mfma_f32_16x16x32_bf16 v[52:55], v[148:151], v[120:123], v[52:55]\n"
    "v_mfma_f32_16x16x32_bf16 v[24:27], v[152:155], v[116:119], v[24:27]\n"
    "s_waitcnt vmcnt(8) lgkmcnt(0)\n"
    "s_barrier\n"
    "s_add_u32 m0, %[m0w], 32768\n"
    "v_mfma_f32_16x16x32_bf16 v[56:59], v[152:155], v[120:123], v[56:59]\n"
    "ds_read_b64_tr_b16 v[128:129], v160 offset:52224\n"
    "ds_read_b64_tr_b16 v[130:131], v160 offset:52480\n"
    "v_mfma_f32_16x16x32_bf16 v[28:31], v[156:159], v[116:119], v[28:31]\n"
    "ds_read_b128 v[116:119], v176 offset:52224\n"
    "buffer_load_dwordx4 v178, %[srdK], s67 offen offset:0 nt lds\n"
    "v_mfma_f32_16x16x32_bf16 v[60:63], v[156:159], v[120:123], v[60:63]\n"
    "ds_read_b128 v[124:127], v177 offset:52224\n"
    "ds_read_b64_tr_b16 v[132:133], v161 offset:52224\n"
    "v_mfma_f32_16x16x32_bf16 v[0:3], v[84:87], v[72:75], v[0:3]\n"
    "ds_read_b64_tr_b16 v[134:135], v161 offset:52480\n"
    "ds_read_b64_tr_b16 v[136:137], v162 offset:52224\n"
    "v_mfma_f32_16x16x32_bf16 v[32:35], v[84:87], v[76:79], v[32:35]\n"
    "ds_read_b64_tr_b16 v[138:139], v162 offset:52480\n"
    "buffer_load_dwordx4 v179, %[srdK], s67 offen offset:2048 nt lds\n"
    "v_mfma_f32_16x16x32_bf16 v[64:67], v[84:87], v[80:83], v[64:67]\n"
    "ds_read_b64_tr_b16 v[140:141], v163 offset:52224\n"
    "ds_read_b64_tr_b16 v[142:143], v163 offset:52480\n"
    "v_mfma_f32_16x16x32_bf16 v[4:7], v[88:91], v[72:75], v[4:7]\n"
    "ds_read_b64_tr_b16 v[144:145], v164 offset:52224\n"
    "v_mfma_f32_16x16x32_bf16 v[36:39], v[88:91], v[76:79], v[36:39]\n"
    "ds_read_b64_tr_b16 v[146:147], v164 offset:52480\n"
    "v_mfma_f32_16x16x32_bf16 v[68:71], v[88:91], v[80:83], v[68:71]\n"
    "ds_read_b128 v[120:123], v176 offset:53248\n"
    "ds_read_b64_tr_b16 v[148:149], v165 offset:52224\n"
    "s_add_u32 m0, %[m0w], 33664\n"
    "s_nop 0\n"
    "buffer_load_dwordx4 v178, %[srdK], s67 offen offset:128 nt lds\n"
    "v_mfma_f32_16x16x32_bf16 v[8:11], v[92:95], v[72:75], v[8:11]\n"
    "ds_read_b64_tr_b16 v[150:151], v165 offset:52480\n"
    "v_mfma_f32_16x16x32_bf16 v[40:43], v[92:95], v[76:79], v[40:43]\n"
    "ds_read_b64_tr_b16 v[152:153], v166 offset:52224\n"
    "ds_read_b64_tr_b16 v[154:155], v166 offset:52480\n"
    "v_mfma_f32_16x16x32_bf16 v[12:15], v[96:99], v[72:75], v[12:15]\n"
    "buffer_load_dwordx4 v179, %[srdK], s67 offen offset:2176 nt lds\n"
    "v_mfma_f32_16x16x32_bf16 v[44:47], v[96:99], v[76:79], v[44:47]\n"
    "ds_read_b64_tr_b16 v[156:157], v167 offset:52224\n"
    "ds_read_b64_tr_b16 v[158:159], v167 offset:52480\n"
    "v_mfma_f32_16x16x32_bf16 v[16:19], v[100:103], v[72:75], v[16:19]\n"
    "v_mfma_f32_16x16x32_bf16 v[48:51], v[100:103], v[76:79], v[48:51]\n"
    "v_mfma_f32_16x16x32_bf16 v[20:23], v[104:107], v[72:75], v[20:23]\n"
    "v_mfma_f32_16x16x32_bf16 v[52:55], v[104:107], v[76:79], v[52:55]\n"
    "v_mfma_f32_16x16x32_bf16 v[24:27], v[108:111], v[72:75], v[24:27]\n"
    "s_waitcnt lgkmcnt(0)\n"
    "v_mfma_f32_16x16x32_bf16 v[56:59], v[108:111], v[76:79], v[56:59]\n"
    "v_mfma_f32_16x16x32_bf16 v[28:31], v[112:115], v[72:75], v[28:31]\n"
    "v_mfma_f32_16x16x32_bf16 v[60:63], v[112:115], v[76:79], v[60:63]\n"
    "v_mfma_f32_16x16x32_bf16 v[0:3], v[128:131], v[116:119], v[0:3]\n"
    "v_mfma_f32_16x16x32_bf16 v[32:35], v[128:131], v[120:123], v[32:35]\n"
    "v_mfma_f32_16x16x32_bf16 v[64:67], v[128:131], v[124:127], v[64:67]\n"
    "v_mfma_f32_16x16x32_bf16 v[4:7], v[132:135], v[116:119], v[4:7]\n"
    "v_mfma_f32_16x16x32_bf16 v[36:39], v[132:135], v[120:123], v[36:39]\n"
    "v_mfma_f32_16x16x32_bf16 v[68:71], v[132:135], v[124:127], v[68:71]\n"
    "v_mfma_f32_16x16x32_bf16 v[8:11], v[136:139], v[116:119], v[8:11]\n"
    "v_mfma_f32_16x16x32_bf16 v[40:43], v[136:139], v[120:123], v[40:43]\n"
    "v_mfma_f32_16x16x32_bf16 v[12:15], v[140:143], v[116:119], v[12:15]\n"
    "v_mfma_f32_16x16x32_bf16 v[44:47], v[140:143], v[120:123], v[44:47]\n"
    "v_mfma_f32_16x16x32_bf16 v[16:19], v[144:147], v[116:119], v[16:19]\n"
    "v_mfma_f32_16x16x32_bf16 v[48:51], v[144:147], v[120:123], v[48:51]\n"
    "v_mfma_f32_16x16x32_bf16 v[20:23], v[148:151], v[116:119], v[20:23]\n"
    "v_mfma_f32_16x16x32_bf16 v[52:55], v[148:151], v[120:123], v[52:55]\n"
    "v_mfma_f32_16x16x32_bf16 v[24:27], v[152:155], v[116:119], v[24:27]\n"
    "v_mfma_f32_16x16x32_bf16 v[56:59], v[152:155], v[120:123], v[56:59]\n"
    "v_mfma_f32_16x16x32_bf16 v[28:31], v[156:159], v[116:119], v[28:31]\n"
    "v_mfma_f32_16x16x32_bf16 v[60:63], v[156:159], v[120:123], v[60:63]\n"
    "s_nop 15\n"
        : "={v[0:31]}"(q01), "={v[32:63]}"(q23), "={v[64:71]}"(qx), [epoch] "=&s"(tag)
        : "{v[176:183]}"(qd), "{v[160:167]}"(qw), "{v[168:175]}"(qs), [srdX] "s"(srdX), [srdW] "s"(srdW), [srdK] "s"(s.srdK), [srdV] "s"(s.srdV), [m0w] "s"(s.m0w), [sow] "s"(s.sow),
          [m0q] "s"(ldsb + 4352u * w), [sx] "s"(sx), [sync] "s"(P.sync)
        : "v72", "v73", "v74", "v75", "v76", "v77", "v78", "v79", "v80", "v81", "v82", "v83", "v84", "v85", "v86", "v87", "v88", "v89", "v90", "v91", "v92", "v93", "v94", "v95", "v96", "v97", "v98", "v99", "v100", "v101", "v102", "v103", "v104", "v105", "v106", "v107", "v108", "v109", "v110", "v111", "v112", "v113", "v114", "v115", "v116", "v117", "v118", "v119", "v120", "v121", "v122", "v123", "v124", "v125", "v126", "v127", "v128", "v129", "v130", "v131", "v132", "v133", "v134", "v135", "v136", "v137", "v138", "v139", "v140", "v141", "v142", "v143", "v144", "v145", "v146", "v147", "v148", "v149", "v150", "v151", "v152", "v153", "v154", "v155", "v156", "v157", "v158", "v159", "s64", "s65", "s66", "s67", "s68", "s69", "s70", "s71", "scc", "memory");
    tag += 1ull;
#pragma unroll
    for (int qt = 0; qt < 2; ++qt)
#pragma unroll
      for (int i = 0; i < 4; ++i)
#pragma unroll
        for (int e = 0; e < 2; ++e) {
          const f32x32& q = qt ? q23 : q01; const int o = 8 * i + 2 * e;
          const auto r = __builtin_amdgcn_permlane32_swap(cvtpk(q[o] * QSCALE, q[o + 1] * QSCALE), cvtpk(q[o + 4] * QSCALE, q[o + 5] * QSCALE), false, false);
          const auto u = __builtin_amdgcn_permlane16_swap(r[0], r[1], false, false);
          qf[16 * qt + 4 * i + e] = u[0]; qf[16 * qt + 4 * i + 2 + e] = u[1];
        }
    asm volatile("s_bitcmp1_b32 %1, 0\n\ts_cbranch_scc0 .La%=\n\t"
                 "v_swap_b32 v0, v4\n\t"
                 "v_swap_b32 v1, v5\n\t"
                 "v_swap_b32 v2, v6\n\t"
                 "v_swap_b32 v3, v7\n\t"
                 "v_swap_b32 v8, v12\n\t"
                 "v_swap_b32 v9, v13\n\t"
                 "v_swap_b32 v10, v14\n\t"
                 "v_swap_b32 v11, v15\n\t"
                 "v_swap_b32 v16, v20\n\t"
                 "v_swap_b32 v17, v21\n\t"
                 "v_swap_b32 v18, v22\n\t"
                 "v_swap_b32 v19, v23\n\t"
                 "v_swap_b32 v24, v28\n\t"
                 "v_swap_b32 v25, v29\n\t"
                 "v_swap_b32 v26, v30\n\t"
                 "v_swap_b32 v27, v31\n\t"
                 ".La%=:\n\ts_bitcmp1_b32 %1, 1\n\ts_cbranch_scc0 .Lb%=\n\t"
                 "v_swap_b32 v0, v8\n\t"
                 "v_swap_b32 v1, v9\n\t"
                 "v_swap_b32 v2, v10\n\t"
                 "v_swap_b32 v3, v11\n\t"
                 "v_swap_b32 v4, v12\n\t"
                 "v_swap_b32 v5, v13\n\t"
                 "v_swap_b32 v6, v14\n\t"
                 "v_swap_b32 v7, v15\n\t"
                 "v_swap_b32 v16, v24\n\t"
                 "v_swap_b32 v17, v25\n\t"
                 "v_swap_b32 v18, v26\n\t"
                 "v_swap_b32 v19, v27\n\t"
                 "v_swap_b32 v20, v28\n\t"
                 "v_swap_b32 v21, v29\n\t"
                 "v_swap_b32 v22, v30\n\t"
                 "v_swap_b32 v23, v31\n\t"
                 ".Lb%=:" : "+{v[0:31]}"(qf) : "s"(w) : "scc");
#pragma unroll
    for (int dt = 0; dt < 2; ++dt) {
      const u32x2 v = {cvtpk(qx[4 * dt] * QSCALE, qx[4 * dt + 1] * QSCALE), cvtpk(qx[4 * dt + 2] * QSCALE, qx[4 * dt + 3] * QSCALE)};
      *(LAS u32x2*)(uintptr_t)(ldsb + QXO + 1024u * w + 16u * (16u * (2u * dt + (g4 >> 1)) + l15) + 8u * (g4 & 1u)) = v;
    }
  }
  STAMP(1);
  set_prio<PRIO0, PRIO1>(role);
  f32x32 oa, ob, oxa; float la0, la1, lx, negmx;
  {
    const unsigned kb = ldsb + 2048u * (l15 >> 3) + 64u * (l15 & 7u) + 16u * (g4 ^ ((0u - (l15 >> 2)) & 3u));
    const unsigned vb = ldsb + 2048u * (g4 >> 1) + 288u * (g4 & 1u) + 64u * tq + 8u * ppos;
    u32x12 ad = lane;
    ad[0] = kb + 4096u * w; ad[1] = kb + 4096u * (w ^ 1u); ad[2] = kb + 4096u * ((w ^ 2u) & 2u);
    ad[3] = vb + 4096u * w; ad[4] = ad[3] ^ 32u; ad[5] = vb + 4096u * (w ^ 1u); ad[6] = ad[5] ^ 32u; ad[7] = vb + 4096u * ((w ^ 2u) & 2u); ad[8] = ad[7] ^ 32u;
    ad[9] = s.kv0; ad[10] = s.kv1; ad[11] = s.vv;
    const u32x4 ones = 0x3f803f80u;
    wait_bar<8>();
    u32x16 qx = lane;
#pragma unroll
    for (int k = 0; k < 4; ++k) {
      const u32x4 v = *(const LAS u32x4*)(uintptr_t)(ldsb + QXO + 1024u * k + 16u * lane);
      qx[4 * k] = v[0]; qx[4 * k + 1] = v[1]; qx[4 * k + 2] = v[2]; qx[4 * k + 3] = v[3];
    }
    asm volatile(
    "v_mov_b32 v252, 0\n"
    "v_mov_b32 v253, 0\n"
    "v_mov_b32 v254, 0\n"
    "v_mov_b32 v255, 0\n"
    "s_nop 1\n"
    "v_mfma_f32_32x32x16_bf16 v[48:63], v[252:255], v[252:255], 0\n"
    "v_mfma_f32_32x32x16_bf16 v[64:79], v[252:255], v[252:255], 0\n"
    "v_mfma_f32_32x32x16_bf16 v[80:95], v[252:255], v[252:255], 0\n"
    "v_mfma_f32_32x32x16_bf16 v[96:111], v[252:255], v[252:255], 0\n"
    "v_mfma_f32_32x32x16_bf16 v[112:127], v[252:255], v[252:255], 0\n"
    "v_mfma_f32_32x32x16_bf16 v[128:143], v[252:255], v[252:255], 0\n"
    "v_mfma_f32_32x32x16_bf16 v[144:159], v[252:255], v[252:255], 0\n"
    "v_mfma_f32_32x32x16_bf16 v[172:187], v[252:255], v[252:255], 0\n"
    "v_mfma_f32_32x32x16_bf16 v[220:235], v[252:255], v[252:255], 0\n"
    "v_mfma_f32_16x16x32_bf16 v[192:195], v[252:255], v[252:255], 0\n"
    "v_mfma_f32_16x16x32_bf16 v[196:199], v[252:255], v[252:255], 0\n"
    "s_mov_b32 s70, %[m0w]\n"
    "s_mov_b32 s71, %[m0w]\n"
    "s_add_u32 s64, %[sow], 0x4000\n"
    "s_add_u32 s65, %[sow], 0x8000\n"
    "s_mov_b32 s66, 8\n"
    "s_mov_b32 s67, 0xff800000\n"
    "s_mov_b32 s68, 0xff800000\n"
    "s_nop 11\n"
    ".Lloop_%=:\n"
    "s_waitcnt vmcnt(8) lgkmcnt(0)\n"
    "s_barrier\n"
    "ds_read_b128 v[204:207], v236 offset:0\n"
    "ds_read_b128 v[208:211], v236 offset:512\n"
    "ds_read_b128 v[212:215], v236 offset:1024\n"
    "ds_read_b128 v[216:219], v236 offset:1536\n"
    "v_mfma_f32_16x16x32_bf16 v[72:75], v[228:231], v[172:175], v[72:75]\n"
    "s_add_u32 m0, %[m0w], 49152\n"
    "v_mfma_f32_16x16x32_bf16 v[104:107], v[228:231], v[176:179], v[104:107]\n"
    "ds_read_b128 v[220:223], v237 offset:0\n"
    "v_mfma_f32_16x16x32_bf16 v[76:79], v[232:235], v[172:175], v[76:79]\n"
    "ds_read_b128 v[224:227], v237 offset:512\n"
    "v_mfma_f32_16x16x32_bf16 v[108:111], v[232:235], v[176:179], v[108:111]\n"
    "ds_read_b128 v[228:231], v237 offset:1024\n"
    "v_mfma_f32_16x16x32_bf16 v[192:195], v[200:203], v[172:175], v[192:195]\n"
    "ds_read_b128 v[232:235], v237 offset:1536\n"
    "v_mfma_f32_16x16x32_bf16 v[196:199], v[200:203], v[176:179], v[196:199]\n"
    "s_waitcnt lgkmcnt(7)\n"
    "v_mfma_f32_16x16x32_bf16 v[156:159], v[204:207], v[0:3], v[144:147]\n"
    "v_mfma_f32_16x16x32_bf16 v[160:163], v[204:207], v[16:19], v[148:151]\n"
    "s_waitcnt vmcnt(4)\n"
    "buffer_load_dwordx4 v247, %[srdV], s64 offen offset:0 nt lds\n"
    "v_mfma_f32_16x16x32_bf16 v[188:191], v[204:207], v[32:35], v[152:155]\n"
    "ds_read_b128 v[204:207], v238 offset:0\n"
    "s_waitcnt lgkmcnt(3)\n"
    "v_mfma_f32_16x16x32_bf16 v[156:159], v[208:211], v[4:7], v[156:159]\n"
    "v_mfma_f32_16x16x32_bf16 v[160:163], v[208:211], v[20:23], v[160:163]\n"
    "v_mfma_f32_16x16x32_bf16 v[188:191], v[208:211], v[36:39], v[188:191]\n"
    "ds_read_b128 v[208:211], v238 offset:512\n"
    "v_mfma_f32_16x16x32_bf16 v[156:159], v[212:215], v[8:11], v[156:159]\n"
    "v_mfma_f32_16x16x32_bf16 v[160:163], v[212:215], v[24:27], v[160:163]\n"
    "s_waitcnt vmcnt(4)\n"
    "buffer_load_dwordx4 v247, %[srdV], s64 offen offset:2048 nt lds\n"
    "v_mfma_f32_16x16x32_bf16 v[188:191], v[212:215], v[40:43], v[188:191]\n"
    "ds_read_b128 v[212:215], v238 offset:1024\n"
    "s_add_u32 m0, %[m0w], 50048\n"
    "v_mfma_f32_16x16x32_bf16 v[156:159], v[216:219], v[12:15], v[156:159]\n"
    "v_mfma_f32_16x16x32_bf16 v[160:163], v[216:219], v[28:31], v[160:163]\n"
    "v_mfma_f32_16x16x32_bf16 v[188:191], v[216:219], v[44:47], v[188:191]\n"
    "ds_read_b128 v[216:219], v238 offset:1536\n"
    "v_mfma_f32_16x16x32_bf16 v[164:167], v[220:223], v[0:3], v[144:147]\n"
    "s_waitcnt vmcnt(4)\n"
    "buffer_load_dwordx4 v247, %[srdV], s64 offen offset:128 nt lds\n"
    "v_mfma_f32_16x16x32_bf16 v[168:171], v[220:223], v[16:19], v[148:151]\n"
    "ds_read_b128 v[220:223], v238 offset:4096\n"
    "v_mfma_f32_16x16x32_bf16 v[164:167], v[224:227], v[4:7], v[164:167]\n"
    "v_mfma_f32_16x16x32_bf16 v[168:171], v[224:227], v[20:23], v[168:171]\n"
    "ds_read_b128 v[224:227], v238 offset:4608\n"
    "s_waitcnt lgkmcnt(3)\n"
    "v_mfma_f32_16x16x32_bf16 v[164:167], v[228:231], v[8:11], v[164:167]\n"
    "v_max3_f32 v248, v156, v157, v158\n"
    "v_mfma_f32_16x16x32_bf16 v[168:171], v[228:231], v[24:27], v[168:171]\n"
    "ds_read_b128 v[228:231], v238 offset:5120\n"
    "v_max3_f32 v249, v159, v160, v161\n"
    "v_mfma_f32_16x16x32_bf16 v[164:167], v[232:235], v[12:15], v[164:167]\n"
    "v_max3_f32 v248, v248, v162, v163\n"
    "v_mfma_f32_16x16x32_bf16 v[168:171], v[232:235], v[28:31], v[168:171]\n"
    "ds_read_b128 v[232:235], v238 offset:5632\n"
    "v_max3_f32 v249, v249, v188, v189\n"
    "v_mfma_f32_16x16x32_bf16 v[172:175], v[204:207], v[0:3], v[144:147]\n"
    "v_max3_f32 v248, v248, v190, v191\n"
    "v_mfma_f32_16x16x32_bf16 v[176:179], v[204:207], v[16:19], v[148:151]\n"
    "s_waitcnt vmcnt(4)\n"
    "buffer_load_dwordx4 v247, %[srdV], s64 offen offset:2176 nt lds\n"
    "s_add_u32 s64, s64, 0x4000\n"
    "v_mfma_f32_16x16x32_bf16 v[172:175], v[208:211], v[4:7], v[172:175]\n"
    "s_nop 0\n"
    "v_mfma_f32_16x16x32_bf16 v[176:179], v[208:211], v[20:23], v[176:179]\n"
    "v_max3_f32 v249, v249, v164, v165\n"
    "v_max3_f32 v248, v248, v166, v167\n"
    "v_mfma_f32_16x16x32_bf16 v[172:175], v[212:215], v[8:11], v[172:175]\n"
    "v_max3_f32 v249, v249, v168, v169\n"
    "v_max3_f32 v248, v248, v170, v171\n"
    "v_mfma_f32_16x16x32_bf16 v[176:179], v[212:215], v[24:27], v[176:179]\n"
    "v_max3_f32 v248, v248, v249, v249\n"
    "v_cmp_lt_f32 vcc, s67, v248\n"
    "s_waitcnt lgkmcnt(0)\n"
    "v_mfma_f32_16x16x32_bf16 v[172:175], v[216:219], v[12:15], v[172:175]\n"
    "v_mfma_f32_16x16x32_bf16 v[176:179], v[216:219], v[28:31], v[176:179]\n"
    "v_mfma_f32_16x16x32_bf16 v[180:183], v[220:223], v[0:3], v[144:147]\n"
    "v_mfma_f32_16x16x32_bf16 v[184:187], v[220:223], v[16:19], v[148:151]\n"
    "s_cbranch_vccnz .Lr0_0_%=_\n"
    ".Lb0_0_%=:\n"
    "v_exp_f32 v156, v156\n"
    "v_exp_f32 v157, v157\n"
    "v_mfma_f32_16x16x32_bf16 v[180:183], v[224:227], v[4:7], v[180:183]\n"
    "v_exp_f32 v158, v158\n"
    "v_exp_f32 v159, v159\n"
    "v_mfma_f32_16x16x32_bf16 v[184:187], v[224:227], v[20:23], v[184:187]\n"
    "v_exp_f32 v164, v164\n"
    "v_exp_f32 v165, v165\n"
    "v_mfma_f32_16x16x32_bf16 v[180:183], v[228:231], v[8:11], v[180:183]\n"
    "v_exp_f32 v166, v166\n"
    "v_exp_f32 v167, v167\n"
    "s_waitcnt vmcnt(8) lgkmcnt(0)\n"
    "s_barrier\n"
    "ds_read_b64_tr_b16 v[204:205], v239 offset:16384\n"
    "ds_read_b64_tr_b16 v[206:207], v241 offset:16384\n"
    "ds_read_b64_tr_b16 v[208:209], v240 offset:16384\n"
    "ds_read_b64_tr_b16 v[210:211], v242 offset:16384\n"
    "v_mfma_f32_16x16x32_bf16 v[184:187], v[228:231], v[24:27], v[184:187]\n"
    "v_cvt_pk_bf16_f32 v156, v156, v157\n"
    "v_cvt_pk_bf16_f32 v157, v158, v159\n"
    "v_exp_f32 v160, v160\n"
    "ds_read_b64_tr_b16 v[212:213], v239 offset:16896\n"
    "ds_read_b64_tr_b16 v[214:215], v241 offset:16896\n"
    "v_mfma_f32_16x16x32_bf16 v[180:183], v[232:235], v[12:15], v[180:183]\n"
    "v_cvt_pk_bf16_f32 v158, v164, v165\n"
    "v_cvt_pk_bf16_f32 v159, v166, v167\n"
    "v_exp_f32 v161, v161\n"
    "v_mfma_f32_16x16x32_bf16 v[184:187], v[232:235], v[28:31], v[184:187]\n"
    "v_exp_f32 v162, v162\n"
    "v_exp_f32 v163, v163\n"
    "ds_read_b64_tr_b16 v[216:217], v240 offset:16896\n"
    "ds_read_b64_tr_b16 v[218:219], v242 offset:16896\n"
    "s_waitcnt lgkmcnt(6)\n"
    "v_mfma_f32_16x16x32_bf16 v[48:51], v[204:207], v[156:159], v[48:51]\n"
    "v_exp_f32 v168, v168\n"
    "v_exp_f32 v169, v169\n"
    "v_mfma_f32_16x16x32_bf16 v[192:195], v[200:203], v[156:159], v[192:195]\n"
    "v_exp_f32 v170, v170\n"
    "v_exp_f32 v171, v171\n"
    "s_waitcnt lgkmcnt(2)\n"
    "v_mfma_f32_16x16x32_bf16 v[52:55], v[208:211], v[156:159], v[52:55]\n"
    "v_cvt_pk_bf16_f32 v160, v160, v161\n"
    "v_cvt_pk_bf16_f32 v161, v162, v163\n"
    "v_exp_f32 v188, v188\n"
    "ds_read_b64_tr_b16 v[220:221], v239 offset:17408\n"
    "ds_read_b64_tr_b16 v[222:223], v241 offset:17408\n"
    "v_mfma_f32_16x16x32_bf16 v[56:59], v[212:215], v[156:159], v[56:59]\n"
    "v_cvt_pk_bf16_f32 v162, v168, v169\n"
    "v_cvt_pk_bf16_f32 v163, v170, v171\n"
    "v_exp_f32 v189, v189\n"
    "v_exp_f32 v190, v190\n"
    "s_waitcnt lgkmcnt(2)\n"
    "v_mfma_f32_16x16x32_bf16 v[60:63], v[216:219], v[156:159], v[60:63]\n"
    "v_exp_f32 v191, v191\n"
    "ds_read_b64_tr_b16 v[224:225], v240 offset:17408\n"
    "ds_read_b64_tr_b16 v[226:227], v242 offset:17408\n"
    "v_mfma_f32_16x16x32_bf16 v[80:83], v[204:207], v[160:163], v[80:83]\n"
    "v_cvt_pk_bf16_f32 v188, v188, v189\n"
    "v_cvt_pk_bf16_f32 v189, v190, v191\n"
    "v_mfma_f32_16x16x32_bf16 v[84:87], v[208:211], v[160:163], v[84:87]\n"
    "v_max3_f32 v248, v172, v173, v174\n"
    "v_max3_f32 v249, v175, v176, v177\n"
    "v_mfma_f32_16x16x16_bf16 v[112:115], v[204:205], v[188:189], v[112:115]\n"
    "ds_read_b64_tr_b16 v[228:229], v239 offset:17920\n"
    "ds_read_b64_tr_b16 v[230:231], v241 offset:17920\n"
    "v_mfma_f32_16x16x16_bf16 v[116:119], v[208:209], v[188:189], v[116:119]\n"
    "v_max3_f32 v248, v248, v178, v179\n"
    "v_max3_f32 v249, v249, v180, v181\n"
    "v_mfma_f32_16x16x32_bf16 v[88:91], v[212:215], v[160:163], v[88:91]\n"
    "ds_read_b64_tr_b16 v[232:233], v240 offset:17920\n"
    "ds_read_b64_tr_b16 v[234:235], v242 offset:17920\n"
    "v_mfma_f32_16x16x16_bf16 v[120:123], v[212:213], v[188:189], v[120:123]\n"
    "v_max3_f32 v248, v248, v182, v183\n"
    "v_max3_f32 v249, v249, v184, v185\n"
    "v_mfma_f32_16x16x32_bf16 v[92:95], v[216:219], v[160:163], v[92:95]\n"
    "ds_read_b64_tr_b16 v[204:205], v243 offset:16384\n"
    "ds_read_b64_tr_b16 v[206:207], v243 offset:20480\n"
    "v_mfma_f32_16x16x16_bf16 v[124:127], v[216:217], v[188:189], v[124:127]\n"
    "v_max3_f32 v248, v248, v186, v187\n"
    "v_max3_f32 v248, v248, v249, v249\n"
    "v_cmp_lt_f32 vcc, 0x41000000, v248\n"
    "v_mfma_f32_16x16x32_bf16 v[196:199], v[200:203], v[160:163], v[196:199]\n"
    "ds_read_b64_tr_b16 v[208:209], v244 offset:16384\n"
    "ds_read_b64_tr_b16 v[210:211], v244 offset:20480\n"
    "v_mfma_f32_16x16x16_bf16 v[252:255], v[200:201], v[188:189], v[252:255]\n"
    "s_add_u32 m0, s70, 0\n"
    "s_waitcnt lgkmcnt(6)\n"
    "v_mfma_f32_16x16x32_bf16 v[64:67], v[220:223], v[156:159], v[64:67]\n"
    "s_waitcnt vmcnt(4)\n"
    "buffer_load_dwordx4 v245, %[srdK], s65 offen offset:0 nt lds\n"
    "s_cbranch_vccnz .Lr1_0_%=_\n"
    ".Lb1_0_%=:\n"
    "v_mfma_f32_16x16x32_bf16 v[96:99], v[220:223], v[160:163], v[96:99]\n"
    "v_exp_f32 v172, v172\n"
    "v_exp_f32 v173, v173\n"
    "v_mfma_f32_16x16x16_bf16 v[128:131], v[220:221], v[188:189], v[128:131]\n"
    "v_exp_f32 v174, v174\n"
    "ds_read_b64_tr_b16 v[212:213], v243 offset:16896\n"
    "ds_read_b64_tr_b16 v[214:215], v243 offset:20992\n"
    "v_mfma_f32_16x16x32_bf16 v[68:71], v[224:227], v[156:159], v[68:71]\n"
    "v_exp_f32 v175, v175\n"
    "v_exp_f32 v180, v180\n"
    "v_mfma_f32_16x16x32_bf16 v[100:103], v[224:227], v[160:163], v[100:103]\n"
    "v_exp_f32 v181, v181\n"
    "v_mfma_f32_16x16x16_bf16 v[132:135], v[224:225], v[188:189], v[132:135]\n"
    "v_exp_f32 v182, v182\n"
    "v_exp_f32 v183, v183\n"
    "ds_read_b64_tr_b16 v[216:217], v244 offset:16896\n"
    "ds_read_b64_tr_b16 v[218:219], v244 offset:20992\n"
    "v_mfma_f32_16x16x32_bf16 v[72:75], v[228:231], v[156:159], v[72:75]\n"
    "v_cvt_pk_bf16_f32 v172, v172, v173\n"
    "v_cvt_pk_bf16_f32 v173, v174, v175\n"
    "v_mfma_f32_16x16x32_bf16 v[104:107], v[228:231], v[160:163], v[104:107]\n"
    "v_cvt_pk_bf16_f32 v174, v180, v181\n"
    "v_cvt_pk_bf16_f32 v175, v182, v183\n"
    "v_mfma_f32_16x16x16_bf16 v[136:139], v[228:229], v[188:189], v[136:139]\n"
    "v_exp_f32 v176, v176\n"
    "v_exp_f32 v177, v177\n"
    "ds_read_b64_tr_b16 v[220:221], v243 offset:17408\n"
    "ds_read_b64_tr_b16 v[222:223], v243 offset:21504\n"
    "s_waitcnt lgkmcnt(4)\n"
    "v_mfma_f32_16x16x32_bf16 v[76:79], v[232:235], v[156:159], v[76:79]\n"
    "v_exp_f32 v178, v178\n"
    "v_exp_f32 v179, v179\n"
    "v_mfma_f32_16x16x32_bf16 v[108:111], v[232:235], v[160:163], v[108:111]\n"
    "v_exp_f32 v184, v184\n"
    "v_exp_f32 v185, v185\n"
    "v_mfma_f32_16x16x16_bf16 v[140:143], v[232:233], v[188:189], v[140:143]\n"
    "ds_read_b64_tr_b16 v[224:225], v244 offset:17408\n"
    "ds_read_b64_tr_b16 v[226:227], v244 offset:21504\n"
    "v_mfma_f32_16x16x32_bf16 v[48:51], v[204:207], v[172:175], v[48:51]\n"
    "v_exp_f32 v186, v186\n"
    "v_exp_f32 v187, v187\n"
    "v_mfma_f32_16x16x32_bf16 v[52:55], v[208:211], v[172:175], v[52:55]\n"
    "ds_read_b64_tr_b16 v[228:229], v243 offset:17920\n"
    "ds_read_b64_tr_b16 v[230:231], v243 offset:22016\n"
    "v_mfma_f32_16x16x32_bf16 v[56:59], v[212:215], v[172:175], v[56:59]\n"
    "v_cvt_pk_bf16_f32 v176, v176, v177\n"
    "v_cvt_pk_bf16_f32 v177, v178, v179\n"
    "s_waitcnt lgkmcnt(4)\n"
    "v_mfma_f32_16x16x32_bf16 v[60:63], v[216:219], v[172:175], v[60:63]\n"
    "v_cvt_pk_bf16_f32 v178, v184, v185\n"
    "v_cvt_pk_bf16_f32 v179, v186, v187\n"
    "ds_read_b64_tr_b16 v[232:233], v244 offset:17920\n"
    "ds_read_b64_tr_b16 v[234:235], v244 offset:22016\n"
    "v_mfma_f32_16x16x32_bf16 v[64:67], v[220:223], v[172:175], v[64:67]\n"
    "s_waitcnt vmcnt(4)\n"
    "buffer_load_dwordx4 v246, %[srdK], s65 offen offset:2048 nt lds\n"
    "s_add_u32 m0, s70, 896\n"
    "v_mfma_f32_16x16x32_bf16 v[80:83], v[204:207], v[176:179], v[80:83]\n"
    "v_mfma_f32_16x16x32_bf16 v[84:87], v[208:211], v[176:179], v[84:87]\n"
    "s_waitcnt vmcnt(4)\n"
    "buffer_load_dwordx4 v245, %[srdK], s65 offen offset:128 nt lds\n"
    "v_mfma_f32_16x16x32_bf16 v[88:91], v[212:215], v[176:179], v[88:91]\n"
    "v_mfma_f32_16x16x32_bf16 v[92:95], v[216:219], v[176:179], v[92:95]\n"
    "s_waitcnt vmcnt(4)\n"
    "buffer_load_dwordx4 v246, %[srdK], s65 offen offset:2176 nt lds\n"
    "s_add_u32 s65, s65, 0x4000\n"
    "v_mfma_f32_16x16x32_bf16 v[96:99], v[220:223], v[176:179], v[96:99]\n"
    "s_waitcnt lgkmcnt(0)\n"
    "v_mfma_f32_16x16x32_bf16 v[68:71], v[224:227], v[172:175], v[68:71]\n"
    "v_mfma_f32_16x16x32_bf16 v[100:103], v[224:227], v[176:179], v[100:103]\n"
    "s_waitcnt vmcnt(8) lgkmcnt(0)\n"
    "s_barrier\n"
    "ds_read_b128 v[204:207], v236 offset:32768\n"
    "ds_read_b128 v[208:211], v236 offset:33280\n"
    "ds_read_b128 v[212:215], v236 offset:33792\n"
    "ds_read_b128 v[216:219], v236 offset:34304\n"
    "v_mfma_f32_16x16x32_bf16 v[72:75], v[228:231], v[172:175], v[72:75]\n"
    "s_add_u32 m0, s71, 16384\n"
    "v_mfma_f32_16x16x32_bf16 v[104:107], v[228:231], v[176:179], v[104:107]\n"
    "ds_read_b128 v[220:223], v237 offset:32768\n"
    "v_mfma_f32_16x16x32_bf16 v[76:79], v[232:235], v[172:175], v[76:79]\n"
    "ds_read_b128 v[224:227], v237 offset:33280\n"
    "v_mfma_f32_16x16x32_bf16 v[108:111], v[232:235], v[176:179], v[108:111]\n"
    "ds_read_b128 v[228:231], v237 offset:33792\n"
    "v_mfma_f32_16x16x32_bf16 v[192:195], v[200:203], v[172:175], v[192:195]\n"
    "ds_read_b128 v[232:235], v237 offset:34304\n"
    "v_mfma_f32_16x16x32_bf16 v[196:199], v[200:203], v[176:179], v[196:199]\n"
    "s_waitcnt lgkmcnt(7)\n"
    "v_mfma_f32_16x16x32_bf16 v[156:159], v[204:207], v[0:3], v[144:147]\n"
    "v_mfma_f32_16x16x32_bf16 v[160:163], v[204:207], v[16:19], v[148:151]\n"
    "s_waitcnt vmcnt(4)\n"
    "buffer_load_dwordx4 v247, %[srdV], s64 offen offset:0 nt lds\n"
    "v_mfma_f32_16x16x32_bf16 v[188:191], v[204:207], v[32:35], v[152:155]\n"
    "ds_read_b128 v[204:207], v238 offset:32768\n"
    "s_waitcnt lgkmcnt(3)\n"
    "v_mfma_f32_16x16x32_bf16 v[156:159], v[208:211], v[4:7], v[156:159]\n"
    "v_mfma_f32_16x16x32_bf16 v[160:163], v[208:211], v[20:23], v[160:163]\n"
    "v_mfma_f32_16x16x32_bf16 v[188:191], v[208:211], v[36:39], v[188:191]\n"
    "ds_read_b128 v[208:211], v238 offset:33280\n"
    "v_mfma_f32_16x16x32_bf16 v[156:159], v[212:215], v[8:11], v[156:159]\n"
    "v_mfma_f32_16x16x32_bf16 v[160:163], v[212:215], v[24:27], v[160:163]\n"
    "s_waitcnt vmcnt(4)\n"
    "buffer_load_dwordx4 v247, %[srdV], s64 offen offset:2048 nt lds\n"
    "v_mfma_f32_16x16x32_bf16 v[188:191], v[212:215], v[40:43], v[188:191]\n"
    "ds_read_b128 v[212:215], v238 offset:33792\n"
    "s_add_u32 m0, s71, 17280\n"
    "v_mfma_f32_16x16x32_bf16 v[156:159], v[216:219], v[12:15], v[156:159]\n"
    "v_mfma_f32_16x16x32_bf16 v[160:163], v[216:219], v[28:31], v[160:163]\n"
    "v_mfma_f32_16x16x32_bf16 v[188:191], v[216:219], v[44:47], v[188:191]\n"
    "ds_read_b128 v[216:219], v238 offset:34304\n"
    "v_mfma_f32_16x16x32_bf16 v[164:167], v[220:223], v[0:3], v[144:147]\n"
    "s_waitcnt vmcnt(4)\n"
    "buffer_load_dwordx4 v247, %[srdV], s64 offen offset:128 nt lds\n"
    "v_mfma_f32_16x16x32_bf16 v[168:171], v[220:223], v[16:19], v[148:151]\n"
    "ds_read_b128 v[220:223], v238 offset:36864\n"
    "v_mfma_f32_16x16x32_bf16 v[164:167], v[224:227], v[4:7], v[164:167]\n"
    "v_mfma_f32_16x16x32_bf16 v[168:171], v[224:227], v[20:23], v[168:171]\n"
    "ds_read_b128 v[224:227], v238 offset:37376\n"
    "s_waitcnt lgkmcnt(3)\n"
    "v_mfma_f32_16x16x32_bf16 v[164:167], v[228:231], v[8:11], v[164:167]\n"
    "v_max3_f32 v248, v156, v157, v158\n"
    "v_mfma_f32_16x16x32_bf16 v[168:171], v[228:231], v[24:27], v[168:171]\n"
    "ds_read_b128 v[228:231], v238 offset:37888\n"
    "v_max3_f32 v249, v159, v160, v161\n"
    "v_mfma_f32_16x16x32_bf16 v[164:167], v[232:235], v[12:15], v[164:167]\n"
    "v_max3_f32 v248, v248, v162, v163\n"
    "v_mfma_f32_16x16x32_bf16 v[168:171], v[232:235], v[28:31], v[168:171]\n"
    "ds_read_b128 v[232:235], v238 offset:38400\n"
    "v_max3_f32 v249, v249, v188, v189\n"
    "v_mfma_f32_16x16x32_bf16 v[172:175], v[204:207], v[0:3], v[144:147]\n"
    "v_max3_f32 v248, v248, v190, v191\n"
    "v_mfma_f32_16x16x32_bf16 v[176:179], v[204:207], v[16:19], v[148:151]\n"
    "s_waitcnt vmcnt(4)\n"
    "buffer_load_dwordx4 v247, %[srdV], s64 offen offset:2176 nt lds\n"
    "s_add_u32 s64, s64, 0x4000\n"
    "v_mfma_f32_16x16x32_bf16 v[172:175], v[208:211], v[4:7], v[172:175]\n"
    "s_nop 0\n"
    "v_mfma_f32_16x16x32_bf16 v[176:179], v[208:211], v[20:23], v[176:179]\n"
    "v_max3_f32 v249, v249, v164, v165\n"
    "v_max3_f32 v248, v248, v166, v167\n"
    "v_mfma_f32_16x16x32_bf16 v[172:175], v[212:215], v[8:11], v[172:175]\n"
    "v_max3_f32 v249, v249, v168, v169\n"
    "v_max3_f32 v248, v248, v170, v171\n"
    "v_mfma_f32_16x16x32_bf16 v[176:179], v[212:215], v[24:27], v[176:179]\n"
    "v_max3_f32 v248, v248, v249, v249\n"
    "v_cmp_lt_f32 vcc, 0x41000000, v248\n"
    "s_waitcnt lgkmcnt(0)\n"
    "v_mfma_f32_16x16x32_bf16 v[172:175], v[216:219], v[12:15], v[172:175]\n"
    "v_mfma_f32_16x16x32_bf16 v[176:179], v[216:219], v[28:31], v[176:179]\n"
    "v_mfma_f32_16x16x32_bf16 v[180:183], v[220:223], v[0:3], v[144:147]\n"
    "v_mfma_f32_16x16x32_bf16 v[184:187], v[220:223], v[16:19], v[148:151]\n"
    "s_cbranch_vccnz .Lr0_1_%=_\n"
    ".Lb0_1_%=:\n"
    "v_exp_f32 v156, v156\n"
    "v_exp_f32 v157, v157\n"
    "v_mfma_f32_16x16x32_bf16 v[180:183], v[224:227], v[4:7], v[180:183]\n"
    "v_exp_f32 v158, v158\n"
    "v_exp_f32 v159, v159\n"
    "v_mfma_f32_16x16x32_bf16 v[184:187], v[224:227], v[20:23], v[184:187]\n"
    "v_exp_f32 v164, v164\n"
    "v_exp_f32 v165, v165\n"
    "v_mfma_f32_16x16x32_bf16 v[180:183], v[228:231], v[8:11], v[180:183]\n"
    "v_exp_f32 v166, v166\n"
    "v_exp_f32 v167, v167\n"
    "s_waitcnt vmcnt(8) lgkmcnt(0)\n"
    "s_barrier\n"
    "ds_read_b64_tr_b16 v[204:205], v239 offset:49152\n"
    "ds_read_b64_tr_b16 v[206:207], v241 offset:49152\n"
    "ds_read_b64_tr_b16 v[208:209], v240 offset:49152\n"
    "ds_read_b64_tr_b16 v[210:211], v242 offset:49152\n"
    "v_mfma_f32_16x16x32_bf16 v[184:187], v[228:231], v[24:27], v[184:187]\n"
    "v_cvt_pk_bf16_f32 v156, v156, v157\n"
    "v_cvt_pk_bf16_f32 v157, v158, v159\n"
    "v_exp_f32 v160, v160\n"
    "ds_read_b64_tr_b16 v[212:213], v239 offset:49664\n"
    "ds_read_b64_tr_b16 v[214:215], v241 offset:49664\n"
    "v_mfma_f32_16x16x32_bf16 v[180:183], v[232:235], v[12:15], v[180:183]\n"
    "v_cvt_pk_bf16_f32 v158, v164, v165\n"
    "v_cvt_pk_bf16_f32 v159, v166, v167\n"
    "v_exp_f32 v161, v161\n"
    "v_mfma_f32_16x16x32_bf16 v[184:187], v[232:235], v[28:31], v[184:187]\n"
    "v_exp_f32 v162, v162\n"
    "v_exp_f32 v163, v163\n"
    "ds_read_b64_tr_b16 v[216:217], v240 offset:49664\n"
    "ds_read_b64_tr_b16 v[218:219], v242 offset:49664\n"
    "s_waitcnt lgkmcnt(6)\n"
    "v_mfma_f32_16x16x32_bf16 v[48:51], v[204:207], v[156:159], v[48:51]\n"
    "v_exp_f32 v168, v168\n"
    "v_exp_f32 v169, v169\n"
    "v_mfma_f32_16x16x32_bf16 v[192:195], v[200:203], v[156:159], v[192:195]\n"
    "v_exp_f32 v170, v170\n"
    "v_exp_f32 v171, v171\n"
    "s_waitcnt lgkmcnt(2)\n"
    "v_mfma_f32_16x16x32_bf16 v[52:55], v[208:211], v[156:159], v[52:55]\n"
    "v_cvt_pk_bf16_f32 v160, v160, v161\n"
    "v_cvt_pk_bf16_f32 v161, v162, v163\n"
    "v_exp_f32 v188, v188\n"
    "ds_read_b64_tr_b16 v[220:221], v239 offset:50176\n"
    "ds_read_b64_tr_b16 v[222:223], v241 offset:50176\n"
    "v_mfma_f32_16x16x32_bf16 v[56:59], v[212:215], v[156:159], v[56:59]\n"
    "v_cvt_pk_bf16_f32 v162, v168, v169\n"
    "v_cvt_pk_bf16_f32 v163, v170, v171\n"
    "v_exp_f32 v189, v189\n"
    "v_exp_f32 v190, v190\n"
    "s_waitcnt lgkmcnt(2)\n"
    "v_mfma_f32_16x16x32_bf16 v[60:63], v[216:219], v[156:159], v[60:63]\n"
    "v_exp_f32 v191, v191\n"
    "ds_read_b64_tr_b16 v[224:225], v240 offset:50176\n"
    "ds_read_b64_tr_b16 v[226:227], v242 offset:50176\n"
    "v_mfma_f32_16x16x32_bf16 v[80:83], v[204:207], v[160:163], v[80:83]\n"
    "v_cvt_pk_bf16_f32 v188, v188, v189\n"
    "v_cvt_pk_bf16_f32 v189, v190, v191\n"
    "v_mfma_f32_16x16x32_bf16 v[84:87], v[208:211], v[160:163], v[84:87]\n"
    "v_max3_f32 v248, v172, v173, v174\n"
    "v_max3_f32 v249, v175, v176, v177\n"
    "v_mfma_f32_16x16x16_bf16 v[112:115], v[204:205], v[188:189], v[112:115]\n"
    "ds_read_b64_tr_b16 v[228:229], v239 offset:50688\n"
    "ds_read_b64_tr_b16 v[230:231], v241 offset:50688\n"
    "v_mfma_f32_16x16x16_bf16 v[116:119], v[208:209], v[188:189], v[116:119]\n"
    "v_max3_f32 v248, v248, v178, v179\n"
    "v_max3_f32 v249, v249, v180, v181\n"
    "v_mfma_f32_16x16x32_bf16 v[88:91], v[212:215], v[160:163], v[88:91]\n"
    "ds_read_b64_tr_b16 v[232:233], v240 offset:50688\n"
    "ds_read_b64_tr_b16 v[234:235], v242 offset:50688\n"
    "v_mfma_f32_16x16x16_bf16 v[120:123], v[212:213], v[188:189], v[120:123]\n"
    "v_max3_f32 v248, v248, v182, v183\n"
    "v_max3_f32 v249, v249, v184, v185\n"
    "v_mfma_f32_16x16x32_bf16 v[92:95], v[216:219], v[160:163], v[92:95]\n"
    "ds_read_b64_tr_b16 v[204:205], v243 offset:49152\n"
    "ds_read_b64_tr_b16 v[206:207], v243 offset:53248\n"
    "v_mfma_f32_16x16x16_bf16 v[124:127], v[216:217], v[188:189], v[124:127]\n"
    "v_max3_f32 v248, v248, v186, v187\n"
    "v_max3_f32 v248, v248, v249, v249\n"
    "v_cmp_lt_f32 vcc, 0x41000000, v248\n"
    "v_mfma_f32_16x16x32_bf16 v[196:199], v[200:203], v[160:163], v[196:199]\n"
    "ds_read_b64_tr_b16 v[208:209], v244 offset:49152\n"
    "ds_read_b64_tr_b16 v[210:211], v244 offset:53248\n"
    "v_mfma_f32_16x16x16_bf16 v[252:255], v[200:201], v[188:189], v[252:255]\n"
    "s_add_u32 m0, s70, 32768\n"
    "s_waitcnt lgkmcnt(6)\n"
    "v_mfma_f32_16x16x32_bf16 v[64:67], v[220:223], v[156:159], v[64:67]\n"
    "s_waitcnt vmcnt(4)\n"
    "buffer_load_dwordx4 v245, %[srdK], s65 offen offset:0 nt lds\n"
    "s_cbranch_vccnz .Lr1_1_%=_\n"
    ".Lb1_1_%=:\n"
    "v_mfma_f32_16x16x32_bf16 v[96:99], v[220:223], v[160:163], v[96:99]\n"
    "v_exp_f32 v172, v172\n"
    "v_exp_f32 v173, v173\n"
    "v_mfma_f32_16x16x16_bf16 v[128:131], v[220:221], v[188:189], v[128:131]\n"
    "v_exp_f32 v174, v174\n"
    "ds_read_b64_tr_b16 v[212:213], v243 offset:49664\n"
    "ds_read_b64_tr_b16 v[214:215], v243 offset:53760\n"
    "v_mfma_f32_16x16x32_bf16 v[68:71], v[224:227], v[156:159], v[68:71]\n"
    "v_exp_f32 v175, v175\n"
    "v_exp_f32 v180, v180\n"
    "v_mfma_f32_16x16x32_bf16 v[100:103], v[224:227], v[160:163], v[100:103]\n"
    "v_exp_f32 v181, v181\n"
    "v_mfma_f32_16x16x16_bf16 v[132:135], v[224:225], v[188:189], v[132:135]\n"
    "v_exp_f32 v182, v182\n"
    "v_exp_f32 v183, v183\n"
    "ds_read_b64_tr_b16 v[216:217], v244 offset:49664\n"
    "ds_read_b64_tr_b16 v[218:219], v244 offset:53760\n"
    "v_mfma_f32_16x16x32_bf16 v[72:75], v[228:231], v[156:159], v[72:75]\n"
    "v_cvt_pk_bf16_f32 v172, v172, v173\n"
    "v_cvt_pk_bf16_f32 v173, v174, v175\n"
    "v_mfma_f32_16x16x32_bf16 v[104:107], v[228:231], v[160:163], v[104:107]\n"
    "v_cvt_pk_bf16_f32 v174, v180, v181\n"
    "v_cvt_pk_bf16_f32 v175, v182, v183\n"
    "v_mfma_f32_16x16x16_bf16 v[136:139], v[228:229], v[188:189], v[136:139]\n"
    "v_exp_f32 v176, v176\n"
    "v_exp_f32 v177, v177\n"
    "ds_read_b64_tr_b16 v[220:221], v243 offset:50176\n"
    "ds_read_b64_tr_b16 v[222:223], v243 offset:54272\n"
    "s_waitcnt lgkmcnt(4)\n"
    "v_mfma_f32_16x16x32_bf16 v[76:79], v[232:235], v[156:159], v[76:79]\n"
    "v_exp_f32 v178, v178\n"
    "v_exp_f32 v179, v179\n"
    "v_mfma_f32_16x16x32_bf16 v[108:111], v[232:235], v[160:163], v[108:111]\n"
    "v_exp_f32 v184, v184\n"
    "v_exp_f32 v185, v185\n"
    "v_mfma_f32_16x16x16_bf16 v[140:143], v[232:233], v[188:189], v[140:143]\n"
    "ds_read_b64_tr_b16 v[224:225], v244 offset:50176\n"
    "ds_read_b64_tr_b16 v[226:227], v244 offset:54272\n"
    "v_mfma_f32_16x16x32_bf16 v[48:51], v[204:207], v[172:175], v[48:51]\n"
    "v_exp_f32 v186, v186\n"
    "v_exp_f32 v187, v187\n"
    "v_mfma_f32_16x16x32_bf16 v[52:55], v[208:211], v[172:175], v[52:55]\n"
    "ds_read_b64_tr_b16 v[228:229], v243 offset:50688\n"
    "ds_read_b64_tr_b16 v[230:231], v243 offset:54784\n"
    "v_mfma_f32_16x16x32_bf16 v[56:59], v[212:215], v[172:175], v[56:59]\n"
    "v_cvt_pk_bf16_f32 v176, v176, v177\n"
    "v_cvt_pk_bf16_f32 v177, v178, v179\n"
    "s_waitcnt lgkmcnt(4)\n"
    "v_mfma_f32_16x16x32_bf16 v[60:63], v[216:219], v[172:175], v[60:63]\n"
    "v_cvt_pk_bf16_f32 v178, v184, v185\n"
    "v_cvt_pk_bf16_f32 v179, v186, v187\n"
    "ds_read_b64_tr_b16 v[232:233], v244 offset:50688\n"
    "ds_read_b64_tr_b16 v[234:235], v244 offset:54784\n"
    "v_mfma_f32_16x16x32_bf16 v[64:67], v[220:223], v[172:175], v[64:67]\n"
    "s_waitcnt vmcnt(4)\n"
    "buffer_load_dwordx4 v246, %[srdK], s65 offen offset:2048 nt lds\n"
    "s_add_u32 m0, s70, 33664\n"
    "v_mfma_f32_16x16x32_bf16 v[80:83], v[204:207], v[176:179], v[80:83]\n"
    "v_mfma_f32_16x16x32_bf16 v[84:87], v[208:211], v[176:179], v[84:87]\n"
    "s_waitcnt vmcnt(4)\n"
    "buffer_load_dwordx4 v245, %[srdK], s65 offen offset:128 nt lds\n"
    "v_mfma_f32_16x16x32_bf16 v[88:91], v[212:215], v[176:179], v[88:91]\n"
    "v_mfma_f32_16x16x32_bf16 v[92:95], v[216:219], v[176:179], v[92:95]\n"
    "s_waitcnt vmcnt(4)\n"
    "buffer_load_dwordx4 v246, %[srdK], s65 offen offset:2176 nt lds\n"
    "s_add_u32 s65, s65, 0x4000\n"
    "s_mov_b32 s67, 0x41000000\n"
    "s_mov_b32 s68, 0\n"
    "s_cmp_eq_u32 s66, 2\n"
    "s_cselect_b32 s70, 0x20000, s70\n"
    "s_mov_b32 s71, s70\n"
    "s_sub_u32 s66, s66, 1\n"
    "s_cmp_lg_u32 s66, 0\n"
    "v_mfma_f32_16x16x32_bf16 v[96:99], v[220:223], v[176:179], v[96:99]\n"
    "s_waitcnt lgkmcnt(0)\n"
    "v_mfma_f32_16x16x32_bf16 v[68:71], v[224:227], v[172:175], v[68:71]\n"
    "v_mfma_f32_16x16x32_bf16 v[100:103], v[224:227], v[176:179], v[100:103]\n"
    "s_cbranch_scc1 .Lloop_%=\n"
    "s_waitcnt vmcnt(8) lgkmcnt(0)\n"
    "s_barrier\n"
    "v_mfma_f32_16x16x32_bf16 v[72:75], v[228:231], v[172:175], v[72:75]\n"
    "v_mfma_f32_16x16x32_bf16 v[104:107], v[228:231], v[176:179], v[104:107]\n"
    "v_mfma_f32_16x16x32_bf16 v[76:79], v[232:235], v[172:175], v[76:79]\n"
    "v_mfma_f32_16x16x32_bf16 v[108:111], v[232:235], v[176:179], v[108:111]\n"
    "v_mfma_f32_16x16x32_bf16 v[192:195], v[200:203], v[172:175], v[192:195]\n"
    "v_mfma_f32_16x16x32_bf16 v[196:199], v[200:203], v[176:179], v[196:199]\n"
    "s_nop 15\n"
    "s_branch .Lend_%=\n"
    ".Lr0_0_%=_:\n"
    "s_nop 15\n"
    "s_nop 15\n"
    "v_max3_f32 v248, v156, v157, v158\n"
    "v_max_f32 v248, v248, v159\n"
    "v_max_f32 v248, v248, v164\n"
    "v_max_f32 v248, v248, v165\n"
    "v_max_f32 v248, v248, v166\n"
    "v_max_f32 v248, v248, v167\n"
    "v_mov_b32 v249, v248\n"
    "s_nop 1\n"
    "v_permlane16_swap_b32 v248, v249\n"
    "s_nop 1\n"
    "v_max_f32 v248, v248, v249\n"
    "v_mov_b32 v249, v248\n"
    "s_nop 1\n"
    "v_permlane32_swap_b32 v248, v249\n"
    "s_nop 1\n"
    "v_max_f32 v248, v248, v249\n"
    "v_ceil_f32 v248, v248\n"
    "v_max_f32 v248, s68, v248\n"
    "v_max_f32 v249, 0, v248\n"
    "v_exp_f32 v249, -v249\n"
    "s_nop 1\n"
    "v_sub_f32 v156, v156, v248\n"
    "v_sub_f32 v157, v157, v248\n"
    "v_sub_f32 v158, v158, v248\n"
    "v_sub_f32 v159, v159, v248\n"
    "v_sub_f32 v164, v164, v248\n"
    "v_sub_f32 v165, v165, v248\n"
    "v_sub_f32 v166, v166, v248\n"
    "v_sub_f32 v167, v167, v248\n"
    "v_sub_f32 v172, v172, v248\n"
    "v_sub_f32 v173, v173, v248\n"
    "v_sub_f32 v174, v174, v248\n"
    "v_sub_f32 v175, v175, v248\n"
    "v_sub_f32 v180, v180, v248\n"
    "v_sub_f32 v181, v181, v248\n"
    "v_sub_f32 v182, v182, v248\n"
    "v_sub_f32 v183, v183, v248\n"
    "v_sub_f32 v144, v144, v248\n"
    "v_sub_f32 v145, v145, v248\n"
    "v_sub_f32 v146, v146, v248\n"
    "v_sub_f32 v147, v147, v248\n"
    "s_cmp_lg_u32 s68, 0\n"
    "s_cbranch_scc1 .Lr0_0_%=_0\n"
    "v_mul_f32 v48, v249, v48\n"
    "v_mul_f32 v49, v249, v49\n"
    "v_mul_f32 v50, v249, v50\n"
    "v_mul_f32 v51, v249, v51\n"
    "v_mul_f32 v52, v249, v52\n"
    "v_mul_f32 v53, v249, v53\n"
    "v_mul_f32 v54, v249, v54\n"
    "v_mul_f32 v55, v249, v55\n"
    "v_mul_f32 v56, v249, v56\n"
    "v_mul_f32 v57, v249, v57\n"
    "v_mul_f32 v58, v249, v58\n"
    "v_mul_f32 v59, v249, v59\n"
    "v_mul_f32 v60, v249, v60\n"
    "v_mul_f32 v61, v249, v61\n"
    "v_mul_f32 v62, v249, v62\n"
    "v_mul_f32 v63, v249, v63\n"
    "v_mul_f32 v64, v249, v64\n"
    "v_mul_f32 v65, v249, v65\n"
    "v_mul_f32 v66, v249, v66\n"
    "v_mul_f32 v67, v249, v67\n"
    "v_mul_f32 v68, v249, v68\n"
    "v_mul_f32 v69, v249, v69\n"
    "v_mul_f32 v70, v249, v70\n"
    "v_mul_f32 v71, v249, v71\n"
    "v_mul_f32 v72, v249, v72\n"
    "v_mul_f32 v73, v249, v73\n"
    "v_mul_f32 v74, v249, v74\n"
    "v_mul_f32 v75, v249, v75\n"
    "v_mul_f32 v76, v249, v76\n"
    "v_mul_f32 v77, v249, v77\n"
    "v_mul_f32 v78, v249, v78\n"
    "v_mul_f32 v79, v249, v79\n"
    "v_mul_f32 v192, v249, v192\n"
    "v_mul_f32 v193, v249, v193\n"
    "v_mul_f32 v194, v249, v194\n"
    "v_mul_f32 v195, v249, v195\n"
    ".Lr0_0_%=_0:\n"
    "v_max3_f32 v248, v160, v161, v162\n"
    "v_max_f32 v248, v248, v163\n"
    "v_max_f32 v248, v248, v168\n"
    "v_max_f32 v248, v248, v169\n"
    "v_max_f32 v248, v248, v170\n"
    "v_max_f32 v248, v248, v171\n"
    "v_mov_b32 v249, v248\n"
    "s_nop 1\n"
    "v_permlane16_swap_b32 v248, v249\n"
    "s_nop 1\n"
    "v_max_f32 v248, v248, v249\n"
    "v_mov_b32 v249, v248\n"
    "s_nop 1\n"
    "v_permlane32_swap_b32 v248, v249\n"
    "s_nop 1\n"
    "v_max_f32 v248, v248, v249\n"
    "v_ceil_f32 v248, v248\n"
    "v_max_f32 v248, s68, v248\n"
    "v_max_f32 v249, 0, v248\n"
    "v_exp_f32 v249, -v249\n"
    "s_nop 1\n"
    "v_sub_f32 v160, v160, v248\n"
    "v_sub_f32 v161, v161, v248\n"
    "v_sub_f32 v162, v162, v248\n"
    "v_sub_f32 v163, v163, v248\n"
    "v_sub_f32 v168, v168, v248\n"
    "v_sub_f32 v169, v169, v248\n"
    "v_sub_f32 v170, v170, v248\n"
    "v_sub_f32 v171, v171, v248\n"
    "v_sub_f32 v176, v176, v248\n"
    "v_sub_f32 v177, v177, v248\n"
    "v_sub_f32 v178, v178, v248\n"
    "v_sub_f32 v179, v179, v248\n"
    "v_sub_f32 v184, v184, v248\n"
    "v_sub_f32 v185, v185, v248\n"
    "v_sub_f32 v186, v186, v248\n"
    "v_sub_f32 v187, v187, v248\n"
    "v_sub_f32 v148, v148, v248\n"
    "v_sub_f32 v149, v149, v248\n"
    "v_sub_f32 v150, v150, v248\n"
    "v_sub_f32 v151, v151, v248\n"
    "s_cmp_lg_u32 s68, 0\n"
    "s_cbranch_scc1 .Lr0_0_%=_1\n"
    "v_mul_f32 v80, v249, v80\n"
    "v_mul_f32 v81, v249, v81\n"
    "v_mul_f32 v82, v249, v82\n"
    "v_mul_f32 v83, v249, v83\n"
    "v_mul_f32 v84, v249, v84\n"
    "v_mul_f32 v85, v249, v85\n"
    "v_mul_f32 v86, v249, v86\n"
    "v_mul_f32 v87, v249, v87\n"
    "v_mul_f32 v88, v249, v88\n"
    "v_mul_f32 v89, v249, v89\n"
    "v_mul_f32 v90, v249, v90\n"
    "v_mul_f32 v91, v249, v91\n"
    "v_mul_f32 v92, v249, v92\n"
    "v_mul_f32 v93, v249, v93\n"
    "v_mul_f32 v94, v249, v94\n"
    "v_mul_f32 v95, v249, v95\n"
    "v_mul_f32 v96, v249, v96\n"
    "v_mul_f32 v97, v249, v97\n"
    "v_mul_f32 v98, v249, v98\n"
    "v_mul_f32 v99, v249, v99\n"
    "v_mul_f32 v100, v249, v100\n"
    "v_mul_f32 v101, v249, v101\n"
    "v_mul_f32 v102, v249, v102\n"
    "v_mul_f32 v103, v249, v103\n"
    "v_mul_f32 v104, v249, v104\n"
    "v_mul_f32 v105, v249, v105\n"
    "v_mul_f32 v106, v249, v106\n"
    "v_mul_f32 v107, v249, v107\n"
    "v_mul_f32 v108, v249, v108\n"
    "v_mul_f32 v109, v249, v109\n"
    "v_mul_f32 v110, v249, v110\n"
    "v_mul_f32 v111, v249, v111\n"
    "v_mul_f32 v196, v249, v196\n"
    "v_mul_f32 v197, v249, v197\n"
    "v_mul_f32 v198, v249, v198\n"
    "v_mul_f32 v199, v249, v199\n"
    ".Lr0_0_%=_1:\n"
    "v_max3_f32 v248, v188, v189, v190\n"
    "v_max_f32 v248, v248, v191\n"
    "v_mov_b32 v249, v248\n"
    "s_nop 1\n"
    "v_permlane16_swap_b32 v248, v249\n"
    "s_nop 1\n"
    "v_max_f32 v248, v248, v249\n"
    "v_mov_b32 v249, v248\n"
    "s_nop 1\n"
    "v_permlane32_swap_b32 v248, v249\n"
    "s_nop 1\n"
    "v_max_f32 v248, v248, v249\n"
    "v_ceil_f32 v248, v248\n"
    "v_max_f32 v248, s68, v248\n"
    "v_max_f32 v249, 0, v248\n"
    "v_exp_f32 v249, -v249\n"
    "s_nop 1\n"
    "v_sub_f32 v188, v188, v248\n"
    "v_sub_f32 v189, v189, v248\n"
    "v_sub_f32 v190, v190, v248\n"
    "v_sub_f32 v191, v191, v248\n"
    "v_sub_f32 v152, v152, v248\n"
    "v_sub_f32 v153, v153, v248\n"
    "v_sub_f32 v154, v154, v248\n"
    "v_sub_f32 v155, v155, v248\n"
    "s_cmp_lg_u32 s68, 0\n"
    "s_cbranch_scc1 .Lr0_0_%=_x\n"
    "v_mul_f32 v112, v249, v112\n"
    "v_mul_f32 v113, v249, v113\n"
    "v_mul_f32 v114, v249, v114\n"
    "v_mul_f32 v115, v249, v115\n"
    "v_mul_f32 v116, v249, v116\n"
    "v_mul_f32 v117, v249, v117\n"
    "v_mul_f32 v118, v249, v118\n"
    "v_mul_f32 v119, v249, v119\n"
    "v_mul_f32 v120, v249, v120\n"
    "v_mul_f32 v121, v249, v121\n"
    "v_mul_f32 v122, v249, v122\n"
    "v_mul_f32 v123, v249, v123\n"
    "v_mul_f32 v124, v249, v124\n"
    "v_mul_f32 v125, v249, v125\n"
    "v_mul_f32 v126, v249, v126\n"
    "v_mul_f32 v127, v249, v127\n"
    "v_mul_f32 v128, v249, v128\n"
    "v_mul_f32 v129, v249, v129\n"
    "v_mul_f32 v130, v249, v130\n"
    "v_mul_f32 v131, v249, v131\n"
    "v_mul_f32 v132, v249, v132\n"
    "v_mul_f32 v133, v249, v133\n"
    "v_mul_f32 v134, v249, v134\n"
    "v_mul_f32 v135, v249, v135\n"
    "v_mul_f32 v136, v249, v136\n"
    "v_mul_f32 v137, v249, v137\n"
    "v_mul_f32 v138, v249, v138\n"
    "v_mul_f32 v139, v249, v139\n"
    "v_mul_f32 v140, v249, v140\n"
    "v_mul_f32 v141, v249, v141\n"
    "v_mul_f32 v142, v249, v142\n"
    "v_mul_f32 v143, v249, v143\n"
    "v_mul_f32 v252, v249, v252\n"
    "v_mul_f32 v253, v249, v253\n"
    "v_mul_f32 v254, v249, v254\n"
    "v_mul_f32 v255, v249, v255\n"
    ".Lr0_0_%=_x:\n"
    "s_nop 7\n"
    "s_branch .Lb0_0_%=\n"
    ".Lr1_0_%=_:\n"
    "s_nop 15\n"
    "s_nop 15\n"
    "v_max3_f32 v248, v172, v173, v174\n"
    "v_max_f32 v248, v248, v175\n"
    "v_max_f32 v248, v248, v180\n"
    "v_max_f32 v248, v248, v181\n"
    "v_max_f32 v248, v248, v182\n"
    "v_max_f32 v248, v248, v183\n"
    "v_mov_b32 v249, v248\n"
    "s_nop 1\n"
    "v_permlane16_swap_b32 v248, v249\n"
    "s_nop 1\n"
    "v_max_f32 v248, v248, v249\n"
    "v_mov_b32 v249, v248\n"
    "s_nop 1\n"
    "v_permlane32_swap_b32 v248, v249\n"
    "s_nop 1\n"
    "v_max_f32 v248, v248, v249\n"
    "v_ceil_f32 v248, v248\n"
    "v_max_f32 v248, 0, v248\n"
    "v_max_f32 v249, 0, v248\n"
    "v_exp_f32 v249, -v249\n"
    "s_nop 1\n"
    "v_sub_f32 v172, v172, v248\n"
    "v_sub_f32 v173, v173, v248\n"
    "v_sub_f32 v174, v174, v248\n"
    "v_sub_f32 v175, v175, v248\n"
    "v_sub_f32 v180, v180, v248\n"
    "v_sub_f32 v181, v181, v248\n"
    "v_sub_f32 v182, v182, v248\n"
    "v_sub_f32 v183, v183, v248\n"
    "v_and_b32 v164, 0xffff0000, v156\n"
    "v_lshlrev_b32 v156, 16, v156\n"
    "v_mul_f32 v164, v249, v164\n"
    "v_mul_f32 v156, v249, v156\n"
    "v_cvt_pk_bf16_f32 v156, v156, v164\n"
    "v_and_b32 v164, 0xffff0000, v157\n"
    "v_lshlrev_b32 v157, 16, v157\n"
    "v_mul_f32 v164, v249, v164\n"
    "v_mul_f32 v157, v249, v157\n"
    "v_cvt_pk_bf16_f32 v157, v157, v164\n"
    "v_and_b32 v164, 0xffff0000, v158\n"
    "v_lshlrev_b32 v158, 16, v158\n"
    "v_mul_f32 v164, v249, v164\n"
    "v_mul_f32 v158, v249, v158\n"
    "v_cvt_pk_bf16_f32 v158, v158, v164\n"
    "v_and_b32 v164, 0xffff0000, v159\n"
    "v_lshlrev_b32 v159, 16, v159\n"
    "v_mul_f32 v164, v249, v164\n"
    "v_mul_f32 v159, v249, v159\n"
    "v_cvt_pk_bf16_f32 v159, v159, v164\n"
    "v_sub_f32 v144, v144, v248\n"
    "v_sub_f32 v145, v145, v248\n"
    "v_sub_f32 v146, v146, v248\n"
    "v_sub_f32 v147, v147, v248\n"
    "v_mul_f32 v48, v249, v48\n"
    "v_mul_f32 v49, v249, v49\n"
    "v_mul_f32 v50, v249, v50\n"
    "v_mul_f32 v51, v249, v51\n"
    "v_mul_f32 v52, v249, v52\n"
    "v_mul_f32 v53, v249, v53\n"
    "v_mul_f32 v54, v249, v54\n"
    "v_mul_f32 v55, v249, v55\n"
    "v_mul_f32 v56, v249, v56\n"
    "v_mul_f32 v57, v249, v57\n"
    "v_mul_f32 v58, v249, v58\n"
    "v_mul_f32 v59, v249, v59\n"
    "v_mul_f32 v60, v249, v60\n"
    "v_mul_f32 v61, v249, v61\n"
    "v_mul_f32 v62, v249, v62\n"
    "v_mul_f32 v63, v249, v63\n"
    "v_mul_f32 v64, v249, v64\n"
    "v_mul_f32 v65, v249, v65\n"
    "v_mul_f32 v66, v249, v66\n"
    "v_mul_f32 v67, v249, v67\n"
    "v_mul_f32 v68, v249, v68\n"
    "v_mul_f32 v69, v249, v69\n"
    "v_mul_f32 v70, v249, v70\n"
    "v_mul_f32 v71, v249, v71\n"
    "v_mul_f32 v72, v249, v72\n"
    "v_mul_f32 v73, v249, v73\n"
    "v_mul_f32 v74, v249, v74\n"
    "v_mul_f32 v75, v249, v75\n"
    "v_mul_f32 v76, v249, v76\n"
    "v_mul_f32 v77, v249, v77\n"
    "v_mul_f32 v78, v249, v78\n"
    "v_mul_f32 v79, v249, v79\n"
    "v_mul_f32 v192, v249, v192\n"
    "v_mul_f32 v193, v249, v193\n"
    "v_mul_f32 v194, v249, v194\n"
    "v_mul_f32 v195, v249, v195\n"
    ".Lr1_0_%=_0:\n"
    "v_max3_f32 v248, v176, v177, v178\n"
    "v_max_f32 v248, v248, v179\n"
    "v_max_f32 v248, v248, v184\n"
    "v_max_f32 v248, v248, v185\n"
    "v_max_f32 v248, v248, v186\n"
    "v_max_f32 v248, v248, v187\n"
    "v_mov_b32 v249, v248\n"
    "s_nop 1\n"
    "v_permlane16_swap_b32 v248, v249\n"
    "s_nop 1\n"
    "v_max_f32 v248, v248, v249\n"
    "v_mov_b32 v249, v248\n"
    "s_nop 1\n"
    "v_permlane32_swap_b32 v248, v249\n"
    "s_nop 1\n"
    "v_max_f32 v248, v248, v249\n"
    "v_ceil_f32 v248, v248\n"
    "v_max_f32 v248, 0, v248\n"
    "v_max_f32 v249, 0, v248\n"
    "v_exp_f32 v249, -v249\n"
    "s_nop 1\n"
    "v_sub_f32 v176, v176, v248\n"
    "v_sub_f32 v177, v177, v248\n"
    "v_sub_f32 v178, v178, v248\n"
    "v_sub_f32 v179, v179, v248\n"
    "v_sub_f32 v184, v184, v248\n"
    "v_sub_f32 v185, v185, v248\n"
    "v_sub_f32 v186, v186, v248\n"
    "v_sub_f32 v187, v187, v248\n"
    "v_and_b32 v164, 0xffff0000, v160\n"
    "v_lshlrev_b32 v160, 16, v160\n"
    "v_mul_f32 v164, v249, v164\n"
    "v_mul_f32 v160, v249, v160\n"
    "v_cvt_pk_bf16_f32 v160, v160, v164\n"
    "v_and_b32 v164, 0xffff0000, v161\n"
    "v_lshlrev_b32 v161, 16, v161\n"
    "v_mul_f32 v164, v249, v164\n"
    "v_mul_f32 v161, v249, v161\n"
    "v_cvt_pk_bf16_f32 v161, v161, v164\n"
    "v_and_b32 v164, 0xffff0000, v162\n"
    "v_lshlrev_b32 v162, 16, v162\n"
    "v_mul_f32 v164, v249, v164\n"
    "v_mul_f32 v162, v249, v162\n"
    "v_cvt_pk_bf16_f32 v162, v162, v164\n"
    "v_and_b32 v164, 0xffff0000, v163\n"
    "v_lshlrev_b32 v163, 16, v163\n"
    "v_mul_f32 v164, v249, v164\n"
    "v_mul_f32 v163, v249, v163\n"
    "v_cvt_pk_bf16_f32 v163, v163, v164\n"
    "v_sub_f32 v148, v148, v248\n"
    "v_sub_f32 v149, v149, v248\n"
    "v_sub_f32 v150, v150, v248\n"
    "v_sub_f32 v151, v151, v248\n"
    "v_mul_f32 v80, v249, v80\n"
    "v_mul_f32 v81, v249, v81\n"
    "v_mul_f32 v82, v249, v82\n"
    "v_mul_f32 v83, v249, v83\n"
    "v_mul_f32 v84, v249, v84\n"
    "v_mul_f32 v85, v249, v85\n"
    "v_mul_f32 v86, v249, v86\n"
    "v_mul_f32 v87, v249, v87\n"
    "v_mul_f32 v88, v249, v88\n"
    "v_mul_f32 v89, v249, v89\n"
    "v_mul_f32 v90, v249, v90\n"
    "v_mul_f32 v91, v249, v91\n"
    "v_mul_f32 v92, v249, v92\n"
    "v_mul_f32 v93, v249, v93\n"
    "v_mul_f32 v94, v249, v94\n"
    "v_mul_f32 v95, v249, v95\n"
    "v_mul_f32 v96, v249, v96\n"
    "v_mul_f32 v97, v249, v97\n"
    "v_mul_f32 v98, v249, v98\n"
    "v_mul_f32 v99, v249, v99\n"
    "v_mul_f32 v100, v249, v100\n"
    "v_mul_f32 v101, v249, v101\n"
    "v_mul_f32 v102, v249, v102\n"
    "v_mul_f32 v103, v249, v103\n"
    "v_mul_f32 v104, v249, v104\n"
    "v_mul_f32 v105, v249, v105\n"
    "v_mul_f32 v106, v249, v106\n"
    "v_mul_f32 v107, v249, v107\n"
    "v_mul_f32 v108, v249, v108\n"
    "v_mul_f32 v109, v249, v109\n"
    "v_mul_f32 v110, v249, v110\n"
    "v_mul_f32 v111, v249, v111\n"
    "v_mul_f32 v196, v249, v196\n"
    "v_mul_f32 v197, v249, v197\n"
    "v_mul_f32 v198, v249, v198\n"
    "v_mul_f32 v199, v249, v199\n"
    ".Lr1_0_%=_1:\n"
    "s_nop 7\n"
    "s_branch .Lb1_0_%=\n"
    ".Lr0_1_%=_:\n"
    "s_nop 15\n"
    "s_nop 15\n"
    "v_max3_f32 v248, v156, v157, v158\n"
    "v_max_f32 v248, v248, v159\n"
    "v_max_f32 v248, v248, v164\n"
    "v_max_f32 v248, v248, v165\n"
    "v_max_f32 v248, v248, v166\n"
    "v_max_f32 v248, v248, v167\n"
    "v_mov_b32 v249, v248\n"
    "s_nop 1\n"
    "v_permlane16_swap_b32 v248, v249\n"
    "s_nop 1\n"
    "v_max_f32 v248, v248, v249\n"
    "v_mov_b32 v249, v248\n"
    "s_nop 1\n"
    "v_permlane32_swap_b32 v248, v249\n"
    "s_nop 1\n"
    "v_max_f32 v248, v248, v249\n"
    "v_ceil_f32 v248, v248\n"
    "v_max_f32 v248, 0, v248\n"
    "v_max_f32 v249, 0, v248\n"
    "v_exp_f32 v249, -v249\n"
    "s_nop 1\n"
    "v_sub_f32 v156, v156, v248\n"
    "v_sub_f32 v157, v157, v248\n"
    "v_sub_f32 v158, v158, v248\n"
    "v_sub_f32 v159, v159, v248\n"
    "v_sub_f32 v164, v164, v248\n"
    "v_sub_f32 v165, v165, v248\n"
    "v_sub_f32 v166, v166, v248\n"
    "v_sub_f32 v167, v167, v248\n"
    "v_sub_f32 v172, v172, v248\n"
    "v_sub_f32 v173, v173, v248\n"
    "v_sub_f32 v174, v174, v248\n"
    "v_sub_f32 v175, v175, v248\n"
    "v_sub_f32 v180, v180, v248\n"
    "v_sub_f32 v181, v181, v248\n"
    "v_sub_f32 v182, v182, v248\n"
    "v_sub_f32 v183, v183, v248\n"
    "v_sub_f32 v144, v144, v248\n"
    "v_sub_f32 v145, v145, v248\n"
    "v_sub_f32 v146, v146, v248\n"
    "v_sub_f32 v147, v147, v248\n"
    "v_mul_f32 v48, v249, v48\n"
    "v_mul_f32 v49, v249, v49\n"
    "v_mul_f32 v50, v249, v50\n"
    "v_mul_f32 v51, v249, v51\n"
    "v_mul_f32 v52, v249, v52\n"
    "v_mul_f32 v53, v249, v53\n"
    "v_mul_f32 v54, v249, v54\n"
    "v_mul_f32 v55, v249, v55\n"
    "v_mul_f32 v56, v249, v56\n"
    "v_mul_f32 v57, v249, v57\n"
    "v_mul_f32 v58, v249, v58\n"
    "v_mul_f32 v59, v249, v59\n"
    "v_mul_f32 v60, v249, v60\n"
    "v_mul_f32 v61, v249, v61\n"
    "v_mul_f32 v62, v249, v62\n"
    "v_mul_f32 v63, v249, v63\n"
    "v_mul_f32 v64, v249, v64\n"
    "v_mul_f32 v65, v249, v65\n"
    "v_mul_f32 v66, v249, v66\n"
    "v_mul_f32 v67, v249, v67\n"
    "v_mul_f32 v68, v249, v68\n"
    "v_mul_f32 v69, v249, v69\n"
    "v_mul_f32 v70, v249, v70\n"
    "v_mul_f32 v71, v249, v71\n"
    "v_mul_f32 v72, v249, v72\n"
    "v_mul_f32 v73, v249, v73\n"
    "v_mul_f32 v74, v249, v74\n"
    "v_mul_f32 v75, v249, v75\n"
    "v_mul_f32 v76, v249, v76\n"
    "v_mul_f32 v77, v249, v77\n"
    "v_mul_f32 v78, v249, v78\n"
    "v_mul_f32 v79, v249, v79\n"
    "v_mul_f32 v192, v249, v192\n"
    "v_mul_f32 v193, v249, v193\n"
    "v_mul_f32 v194, v249, v194\n"
    "v_mul_f32 v195, v249, v195\n"
    ".Lr0_1_%=_0:\n"
    "v_max3_f32 v248, v160, v161, v162\n"
    "v_max_f32 v248, v248, v163\n"
    "v_max_f32 v248, v248, v168\n"
    "v_max_f32 v248, v248, v169\n"
    "v_max_f32 v248, v248, v170\n"
    "v_max_f32 v248, v248, v171\n"
    "v_mov_b32 v249, v248\n"
    "s_nop 1\n"
    "v_permlane16_swap_b32 v248, v249\n"
    "s_nop 1\n"
    "v_max_f32 v248, v248, v249\n"
    "v_mov_b32 v249, v248\n"
    "s_nop 1\n"
    "v_permlane32_swap_b32 v248, v249\n"
    "s_nop 1\n"
    "v_max_f32 v248, v248, v249\n"
    "v_ceil_f32 v248, v248\n"
    "v_max_f32 v248, 0, v248\n"
    "v_max_f32 v249, 0, v248\n"
    "v_exp_f32 v249, -v249\n"
    "s_nop 1\n"
    "v_sub_f32 v160, v160, v248\n"
    "v_sub_f32 v161, v161, v248\n"
    "v_sub_f32 v162, v162, v248\n"
    "v_sub_f32 v163, v163, v248\n"
    "v_sub_f32 v168, v168, v248\n"
    "v_sub_f32 v169, v169, v248\n"
    "v_sub_f32 v170, v170, v248\n"
    "v_sub_f32 v171, v171, v248\n"
    "v_sub_f32 v176, v176, v248\n"
    "v_sub_f32 v177, v177, v248\n"
    "v_sub_f32 v178, v178, v248\n"
    "v_sub_f32 v179, v179, v248\n"
    "v_sub_f32 v184, v184, v248\n"
    "v_sub_f32 v185, v185, v248\n"
    "v_sub_f32 v186, v186, v248\n"
    "v_sub_f32 v187, v187, v248\n"
    "v_sub_f32 v148, v148, v248\n"
    "v_sub_f32 v149, v149, v248\n"
    "v_sub_f32 v150, v150, v248\n"
    "v_sub_f32 v151, v151, v248\n"
    "v_mul_f32 v80, v249, v80\n"
    "v_mul_f32 v81, v249, v81\n"
    "v_mul_f32 v82, v249, v82\n"
    "v_mul_f32 v83, v249, v83\n"
    "v_mul_f32 v84, v249, v84\n"
    "v_mul_f32 v85, v249, v85\n"
    "v_mul_f32 v86, v249, v86\n"
    "v_mul_f32 v87, v249, v87\n"
    "v_mul_f32 v88, v249, v88\n"
    "v_mul_f32 v89, v249, v89\n"
    "v_mul_f32 v90, v249, v90\n"
    "v_mul_f32 v91, v249, v91\n"
    "v_mul_f32 v92, v249, v92\n"
    "v_mul_f32 v93, v249, v93\n"
    "v_mul_f32 v94, v249, v94\n"
    "v_mul_f32 v95, v249, v95\n"
    "v_mul_f32 v96, v249, v96\n"
    "v_mul_f32 v97, v249, v97\n"
    "v_mul_f32 v98, v249, v98\n"
    "v_mul_f32 v99, v249, v99\n"
    "v_mul_f32 v100, v249, v100\n"
    "v_mul_f32 v101, v249, v101\n"
    "v_mul_f32 v102, v249, v102\n"
    "v_mul_f32 v103, v249, v103\n"
    "v_mul_f32 v104, v249, v104\n"
    "v_mul_f32 v105, v249, v105\n"
    "v_mul_f32 v106, v249, v106\n"
    "v_mul_f32 v107, v249, v107\n"
    "v_mul_f32 v108, v249, v108\n"
    "v_mul_f32 v109, v249, v109\n"
    "v_mul_f32 v110, v249, v110\n"
    "v_mul_f32 v111, v249, v111\n"
    "v_mul_f32 v196, v249, v196\n"
    "v_mul_f32 v197, v249, v197\n"
    "v_mul_f32 v198, v249, v198\n"
    "v_mul_f32 v199, v249, v199\n"
    ".Lr0_1_%=_1:\n"
    "v_max3_f32 v248, v188, v189, v190\n"
    "v_max_f32 v248, v248, v191\n"
    "v_mov_b32 v249, v248\n"
    "s_nop 1\n"
    "v_permlane16_swap_b32 v248, v249\n"
    "s_nop 1\n"
    "v_max_f32 v248, v248, v249\n"
    "v_mov_b32 v249, v248\n"
    "s_nop 1\n"
    "v_permlane32_swap_b32 v248, v249\n"
    "s_nop 1\n"
    "v_max_f32 v248, v248, v249\n"
    "v_ceil_f32 v248, v248\n"
    "v_max_f32 v248, 0, v248\n"
    "v_max_f32 v249, 0, v248\n"
    "v_exp_f32 v249, -v249\n"
    "s_nop 1\n"
    "v_sub_f32 v188, v188, v248\n"
    "v_sub_f32 v189, v189, v248\n"
    "v_sub_f32 v190, v190, v248\n"
    "v_sub_f32 v191, v191, v248\n"
    "v_sub_f32 v152, v152, v248\n"
    "v_sub_f32 v153, v153, v248\n"
    "v_sub_f32 v154, v154, v248\n"
    "v_sub_f32 v155, v155, v248\n"
    "v_mul_f32 v112, v249, v112\n"
    "v_mul_f32 v113, v249, v113\n"
    "v_mul_f32 v114, v249, v114\n"
    "v_mul_f32 v115, v249, v115\n"
    "v_mul_f32 v116, v249, v116\n"
    "v_mul_f32 v117, v249, v117\n"
    "v_mul_f32 v118, v249, v118\n"
    "v_mul_f32 v119, v249, v119\n"
    "v_mul_f32 v120, v249, v120\n"
    "v_mul_f32 v121, v249, v121\n"
    "v_mul_f32 v122, v249, v122\n"
    "v_mul_f32 v123, v249, v123\n"
    "v_mul_f32 v124, v249, v124\n"
    "v_mul_f32 v125, v249, v125\n"
    "v_mul_f32 v126, v249, v126\n"
    "v_mul_f32 v127, v249, v127\n"
    "v_mul_f32 v128, v249, v128\n"
    "v_mul_f32 v129, v249, v129\n"
    "v_mul_f32 v130, v249, v130\n"
    "v_mul_f32 v131, v249, v131\n"
    "v_mul_f32 v132, v249, v132\n"
    "v_mul_f32 v133, v249, v133\n"
    "v_mul_f32 v134, v249, v134\n"
    "v_mul_f32 v135, v249, v135\n"
    "v_mul_f32 v136, v249, v136\n"
    "v_mul_f32 v137, v249, v137\n"
    "v_mul_f32 v138, v249, v138\n"
    "v_mul_f32 v139, v249, v139\n"
    "v_mul_f32 v140, v249, v140\n"
    "v_mul_f32 v141, v249, v141\n"
    "v_mul_f32 v142, v249, v142\n"
    "v_mul_f32 v143, v249, v143\n"
    "v_mul_f32 v252, v249, v252\n"
    "v_mul_f32 v253, v249, v253\n"
    "v_mul_f32 v254, v249, v254\n"
    "v_mul_f32 v255, v249, v255\n"
    ".Lr0_1_%=_x:\n"
    "s_nop 7\n"
    "s_branch .Lb0_1_%=\n"
    ".Lr1_1_%=_:\n"
    "s_nop 15\n"
    "s_nop 15\n"
    "v_max3_f32 v248, v172, v173, v174\n"
    "v_max_f32 v248, v248, v175\n"
    "v_max_f32 v248, v248, v180\n"
    "v_max_f32 v248, v248, v181\n"
    "v_max_f32 v248, v248, v182\n"
    "v_max_f32 v248, v248, v183\n"
    "v_mov_b32 v249, v248\n"
    "s_nop 1\n"
    "v_permlane16_swap_b32 v248, v249\n"
    "s_nop 1\n"
    "v_max_f32 v248, v248, v249\n"
    "v_mov_b32 v249, v248\n"
    "s_nop 1\n"
    "v_permlane32_swap_b32 v248, v249\n"
    "s_nop 1\n"
    "v_max_f32 v248, v248, v249\n"
    "v_ceil_f32 v248, v248\n"
    "v_max_f32 v248, 0, v248\n"
    "v_max_f32 v249, 0, v248\n"
    "v_exp_f32 v249, -v249\n"
    "s_nop 1\n"
    "v_sub_f32 v172, v172, v248\n"
    "v_sub_f32 v173, v173, v248\n"
    "v_sub_f32 v174, v174, v248\n"
    "v_sub_f32 v175, v175, v248\n"
    "v_sub_f32 v180, v180, v248\n"
    "v_sub_f32 v181, v181, v248\n"
    "v_sub_f32 v182, v182, v248\n"
    "v_sub_f32 v183, v183, v248\n"
    "v_and_b32 v164, 0xffff0000, v156\n"
    "v_lshlrev_b32 v156, 16, v156\n"
    "v_mul_f32 v164, v249, v164\n"
    "v_mul_f32 v156, v249, v156\n"
    "v_cvt_pk_bf16_f32 v156, v156, v164\n"
    "v_and_b32 v164, 0xffff0000, v157\n"
    "v_lshlrev_b32 v157, 16, v157\n"
    "v_mul_f32 v164, v249, v164\n"
    "v_mul_f32 v157, v249, v157\n"
    "v_cvt_pk_bf16_f32 v157, v157, v164\n"
    "v_and_b32 v164, 0xffff0000, v158\n"
    "v_lshlrev_b32 v158, 16, v158\n"
    "v_mul_f32 v164, v249, v164\n"
    "v_mul_f32 v158, v249, v158\n"
    "v_cvt_pk_bf16_f32 v158, v158, v164\n"
    "v_and_b32 v164, 0xffff0000, v159\n"
    "v_lshlrev_b32 v159, 16, v159\n"
    "v_mul_f32 v164, v249, v164\n"
    "v_mul_f32 v159, v249, v159\n"
    "v_cvt_pk_bf16_f32 v159, v159, v164\n"
    "v_sub_f32 v144, v144, v248\n"
    "v_sub_f32 v145, v145, v248\n"
    "v_sub_f32 v146, v146, v248\n"
    "v_sub_f32 v147, v147, v248\n"
    "v_mul_f32 v48, v249, v48\n"
    "v_mul_f32 v49, v249, v49\n"
    "v_mul_f32 v50, v249, v50\n"
    "v_mul_f32 v51, v249, v51\n"
    "v_mul_f32 v52, v249, v52\n"
    "v_mul_f32 v53, v249, v53\n"
    "v_mul_f32 v54, v249, v54\n"
    "v_mul_f32 v55, v249, v55\n"
    "v_mul_f32 v56, v249, v56\n"
    "v_mul_f32 v57, v249, v57\n"
    "v_mul_f32 v58, v249, v58\n"
    "v_mul_f32 v59, v249, v59\n"
    "v_mul_f32 v60, v249, v60\n"
    "v_mul_f32 v61, v249, v61\n"
    "v_mul_f32 v62, v249, v62\n"
    "v_mul_f32 v63, v249, v63\n"
    "v_mul_f32 v64, v249, v64\n"
    "v_mul_f32 v65, v249, v65\n"
    "v_mul_f32 v66, v249, v66\n"
    "v_mul_f32 v67, v249, v67\n"
    "v_mul_f32 v68, v249, v68\n"
    "v_mul_f32 v69, v249, v69\n"
    "v_mul_f32 v70, v249, v70\n"
    "v_mul_f32 v71, v249, v71\n"
    "v_mul_f32 v72, v249, v72\n"
    "v_mul_f32 v73, v249, v73\n"
    "v_mul_f32 v74, v249, v74\n"
    "v_mul_f32 v75, v249, v75\n"
    "v_mul_f32 v76, v249, v76\n"
    "v_mul_f32 v77, v249, v77\n"
    "v_mul_f32 v78, v249, v78\n"
    "v_mul_f32 v79, v249, v79\n"
    "v_mul_f32 v192, v249, v192\n"
    "v_mul_f32 v193, v249, v193\n"
    "v_mul_f32 v194, v249, v194\n"
    "v_mul_f32 v195, v249, v195\n"
    ".Lr1_1_%=_0:\n"
    "v_max3_f32 v248, v176, v177, v178\n"
    "v_max_f32 v248, v248, v179\n"
    "v_max_f32 v248, v248, v184\n"
    "v_max_f32 v248, v248, v185\n"
    "v_max_f32 v248, v248, v186\n"
    "v_max_f32 v248, v248, v187\n"
    "v_mov_b32 v249, v248\n"
    "s_nop 1\n"
    "v_permlane16_swap_b32 v248, v249\n"
    "s_nop 1\n"
    "v_max_f32 v248, v248, v249\n"
    "v_mov_b32 v249, v248\n"
    "s_nop 1\n"
    "v_permlane32_swap_b32 v248, v249\n"
    "s_nop 1\n"
    "v_max_f32 v248, v248, v249\n"
    "v_ceil_f32 v248, v248\n"
    "v_max_f32 v248, 0, v248\n"
    "v_max_f32 v249, 0, v248\n"
    "v_exp_f32 v249, -v249\n"
    "s_nop 1\n"
    "v_sub_f32 v176, v176, v248\n"
    "v_sub_f32 v177, v177, v248\n"
    "v_sub_f32 v178, v178, v248\n"
    "v_sub_f32 v179, v179, v248\n"
    "v_sub_f32 v184, v184, v248\n"
    "v_sub_f32 v185, v185, v248\n"
    "v_sub_f32 v186, v186, v248\n"
    "v_sub_f32 v187, v187, v248\n"
    "v_and_b32 v164, 0xffff0000, v160\n"
    "v_lshlrev_b32 v160, 16, v160\n"
    "v_mul_f32 v164, v249, v164\n"
    "v_mul_f32 v160, v249, v160\n"
    "v_cvt_pk_bf16_f32 v160, v160, v164\n"
    "v_and_b32 v164, 0xffff0000, v161\n"
    "v_lshlrev_b32 v161, 16, v161\n"
    "v_mul_f32 v164, v249, v164\n"
    "v_mul_f32 v161, v249, v161\n"
    "v_cvt_pk_bf16_f32 v161, v161, v164\n"
    "v_and_b32 v164, 0xffff0000, v162\n"
    "v_lshlrev_b32 v162, 16, v162\n"
    "v_mul_f32 v164, v249, v164\n"
    "v_mul_f32 v162, v249, v162\n"
    "v_cvt_pk_bf16_f32 v162, v162, v164\n"
    "v_and_b32 v164, 0xffff0000, v163\n"
    "v_lshlrev_b32 v163, 16, v163\n"
    "v_mul_f32 v164, v249, v164\n"
    "v_mul_f32 v163, v249, v163\n"
    "v_cvt_pk_bf16_f32 v163, v163, v164\n"
    "v_sub_f32 v148, v148, v248\n"
    "v_sub_f32 v149, v149, v248\n"
    "v_sub_f32 v150, v150, v248\n"
    "v_sub_f32 v151, v151, v248\n"
    "v_mul_f32 v80, v249, v80\n"
    "v_mul_f32 v81, v249, v81\n"
    "v_mul_f32 v82, v249, v82\n"
    "v_mul_f32 v83, v249, v83\n"
    "v_mul_f32 v84, v249, v84\n"
    "v_mul_f32 v85, v249, v85\n"
    "v_mul_f32 v86, v249, v86\n"
    "v_mul_f32 v87, v249, v87\n"
    "v_mul_f32 v88, v249, v88\n"
    "v_mul_f32 v89, v249, v89\n"
    "v_mul_f32 v90, v249, v90\n"
    "v_mul_f32 v91, v249, v91\n"
    "v_mul_f32 v92, v249, v92\n"
    "v_mul_f32 v93, v249, v93\n"
    "v_mul_f32 v94, v249, v94\n"
    "v_mul_f32 v95, v249, v95\n"
    "v_mul_f32 v96, v249, v96\n"
    "v_mul_f32 v97, v249, v97\n"
    "v_mul_f32 v98, v249, v98\n"
    "v_mul_f32 v99, v249, v99\n"
    "v_mul_f32 v100, v249, v100\n"
    "v_mul_f32 v101, v249, v101\n"
    "v_mul_f32 v102, v249, v102\n"
    "v_mul_f32 v103, v249, v103\n"
    "v_mul_f32 v104, v249, v104\n"
    "v_mul_f32 v105, v249, v105\n"
    "v_mul_f32 v106, v249, v106\n"
    "v_mul_f32 v107, v249, v107\n"
    "v_mul_f32 v108, v249, v108\n"
    "v_mul_f32 v109, v249, v109\n"
    "v_mul_f32 v110, v249, v110\n"
    "v_mul_f32 v111, v249, v111\n"
    "v_mul_f32 v196, v249, v196\n"
    "v_mul_f32 v197, v249, v197\n"
    "v_mul_f32 v198, v249, v198\n"
    "v_mul_f32 v199, v249, v199\n"
    ".Lr1_1_%=_1:\n"
    "s_nop 7\n"
    "s_branch .Lb1_1_%=\n"
    ".Lend_%=:\n"
        : "={v[48:79]}"(oa), "={v[80:111]}"(ob), "={v[112:143]}"(oxa), "={v192}"(la0), "={v196}"(la1), "={v252}"(lx), "={v152}"(negmx)
        : "{v[0:31]}"(qf), "{v[32:47]}"(qx), "{v[236:247]}"(ad), "{v[200:203]}"(ones), [srdK] "s"(s.srdK), [srdV] "s"(s.srdV), [m0w] "s"(s.m0w), [sow] "s"(s.sow), [srdP] "s"(srdP)
        : "v144", "v145", "v146", "v147", "v148", "v149", "v150", "v151", "v153", "v154", "v155", "v156", "v157", "v158", "v159", "v160", "v161", "v162", "v163", "v164", "v165", "v166", "v167", "v168", "v169", "v170", "v171", "v172", "v173", "v174", "v175", "v176", "v177", "v178", "v179", "v180", "v181", "v182", "v183", "v184", "v185", "v186", "v187", "v188", "v189", "v190", "v191", "v193", "v194", "v195", "v197", "v198", "v199", "v204", "v205", "v206", "v207", "v208", "v209", "v210", "v211", "v212", "v213", "v214", "v215", "v216", "v217", "v218", "v219", "v220", "v221", "v222", "v223", "v224", "v225", "v226", "v227", "v228", "v229", "v230", "v231", "v232", "v233", "v234", "v235", "v248", "v249", "v253", "v254", "v255", "s64", "s65", "s66", "s67", "s68", "s70", "s71", "vcc", "scc", "memory");
  }
  STAMP(2);
  unsigned ln;
  asm volatile("v_mbcnt_lo_u32_b32 %0, -1, 0\n\tv_mbcnt_hi_u32_b32 %0, -1, %0" : "=v"(ln));
  const unsigned e15 = ln & 15u, eg4 = ln >> 4, ehh = ln >> 5, eds = (ln >> 4) & 1u;
  rsrc_t srdO = __builtin_amdgcn_make_buffer_rsrc((void*)(P.ot + (size_t)br * (36u * 256u * 1024u)), 0, (36 * 256 * 1024), 0x00020000);
  {
    const float i0 = __builtin_amdgcn_rcpf(la0), i1 = __builtin_amdgcn_rcpf(la1);
    const unsigned so = ((9u * b + 2u * w) * 128u + 4u * h) * 1024u + 16u * ln;
#pragma unroll
    for (int f = 0; f < 4; ++f) {
      const int o = 8 * f;
      const u32x4 x = {cvtpk(oa[o] * i0, oa[o + 1] * i0), cvtpk(oa[o + 2] * i0, oa[o + 3] * i0), cvtpk(oa[o + 4] * i0, oa[o + 5] * i0), cvtpk(oa[o + 6] * i0, oa[o + 7] * i0)};
      const u32x4 y = {cvtpk(ob[o] * i1, ob[o + 1] * i1), cvtpk(ob[o + 2] * i1, ob[o + 3] * i1), cvtpk(ob[o + 4] * i1, ob[o + 5] * i1), cvtpk(ob[o + 6] * i1, ob[o + 7] * i1)};
      __builtin_amdgcn_raw_buffer_store_b128(x, srdO, so + 1024u * f, 0, OAUX);
      __builtin_amdgcn_raw_buffer_store_b128(y, srdO, so + 1024u * f + 131072u, 0, OAUX);
    }
  }
  {
    const float mw = -negmx;
    const float lw = lx;
    STAMP(11);
    LAS f32x2* sm = (LAS f32x2*)(uintptr_t)(ldsb + 40960u);
    if (ln < 16u) sm[16u * w + ln] = (f32x2){mw, lw};
#pragma unroll
    for (int dt = 0; dt < 8; ++dt) *(LAS f32x4*)(uintptr_t)(ldsb + 8448u * w + 528u * e15 + 64u * dt + 16u * eg4) = (f32x4){oxa[4 * dt], oxa[4 * dt + 1], oxa[4 * dt + 2], oxa[4 * dt + 3]};
    lds_bar();
    f32x2 ml[4];
#pragma unroll
    for (int v = 0; v < 4; ++v) ml[v] = sm[16u * v + e15];
    const float M = fmx(fmx(ml[0][0], ml[1][0]), fmx(ml[2][0], ml[3][0]));
    f32x4 a = {}, c = {}; float L = 0.f;
    const unsigned ra = ldsb + 528u * e15 + 128u * w + 16u * eg4;
#pragma unroll
    for (int v = 0; v < 4; ++v) {
      const float f = __builtin_amdgcn_exp2f(ml[v][0] - M);
      L += f * ml[v][1]; a += f * *(const LAS f32x4*)(uintptr_t)(ra + 8448u * v); c += f * *(const LAS f32x4*)(uintptr_t)(ra + 8448u * v + 64u);
    }
    const float inv = __builtin_amdgcn_rcpf(L);
    a *= inv; c *= inv;
    const u32x4 r = {cvtpk(a[0], a[1]), cvtpk(a[2], a[3]), cvtpk(c[0], c[1]), cvtpk(c[2], c[3])};
    __builtin_amdgcn_raw_buffer_store_b128(r, srdO, ((9u * b + 8u) * 128u + 4u * h + w) * 1024u + 16u * ln, 0, OAUX);
  }
  STAMP(12);
  wait_bar<0>();
  LAS unsigned* bc = (LAS unsigned*)(uintptr_t)ldsb;
  if (w == 0) {
    const unsigned l2 = lane_id();
    if (!OREAD && l2 == 0) __hip_atomic_store(P.sync + 64u + (((br * 8u + b) * 2u + ((h >> 2) & 1u)) * 16u + 4u * (h >> 3) + (h & 3u)), tag, __ATOMIC_RELAXED, __HIP_MEMORY_SCOPE_AGENT);
  }
  const unsigned tile = i5 + 32u * (role ^ OWNROLE);
  {
    asm volatile("buffer_load_dword v255, %0, %1, 0 offen" :: "v"(128u * (64u * w + lane_id())), "s"(srdP) : "v255", "memory");
  }
  STAMP(3);
  if (tile < 30u) oproj_phase(P, ldsb, w, g, tile, tag);
  if (p == (DYNCLAIM ? 511u : 255u + 256u * OWNROLE) && w == 0) {
    const unsigned l2 = lane_id();
    if (wait_tags<8>(P.sync + 64u, l2, tag)) { if (l2 == 0) __hip_atomic_store(P.sync, tag, __ATOMIC_RELAXED, __HIP_MEMORY_SCOPE_AGENT); }
  }
}

extern "C" void kernel_launch(void* const* d_in, const int* in_sizes, int n_in, void* d_out, int out_size, void* d_ws, size_t ws_size, hipStream_t stream) {
  (void)in_sizes; (void)n_in; (void)out_size;
  Params P;
  for (int i = 0; i < 2; ++i) {
    P.x[i] = (const char*)d_in[5 * i]; P.k[i] = (const char*)d_in[5 * i + 1]; P.v[i] = (const char*)d_in[5 * i + 2];
    P.wq[i] = (const char*)d_in[5 * i + 3]; P.wo[i] = (const char*)d_in[5 * i + 4];
  }
  P.sync = (unsigned long long*)d_ws; P.ot = (char*)d_ws + 65536; P.out = (unsigned short*)d_out;
  P.dbg = (unsigned long long*)((char*)d_ws + ws_size - 512 * 16 * 8);
  (void)hipFuncSetAttribute((const void*)attn_kernel, hipFuncAttributeMaxDynamicSharedMemorySize, LDS1);
  attn_kernel<<<512, 256, LDS1, stream>>>(P);
}
```
